```python
import torch

def _softmax(x, axis=-1):
    return torch.softmax(x, dim=axis)
def _gelu(x):
    return torch.nn.functional.gelu(x, approximate="tanh")
def _silu(x):
    return torch.nn.functional.silu(x)
def _sigmoid(x):
    return torch.sigmoid(x)
def _layernorm(x, axis=-1, eps=1e-5):
    m = x.mean(dim=axis, keepdim=True)
    v = x.var(dim=axis, keepdim=True, unbiased=False)
    return (x - m) / torch.sqrt(v + eps)
def _scatter_add(tgt, idx, src, axis=0):
    return tgt.clone().index_add_(axis, idx, src)
def _segment_sum(src, seg, n):
    out = torch.zeros((n,) + src.shape[1:], dtype=src.dtype)
    return out.index_add_(0, seg, src)
def _pad(x, pw):
    flat = tuple(v for pair in reversed(pw) for v in pair)
    return torch.nn.functional.pad(x, flat)

kernel_name = 'parallel_sum__mha_block__dense_activation_relu__prompt'

def setup_inputs():

    _w = (lambda a: (lambda t: t.to(torch.bfloat16) if t.is_floating_point() else t)(torch.as_tensor(a)))
    def _A_setup():
        B, S, D, H = 1, 2048, 512, 32
        return {
            "x": _w((lambda *s: torch.randn(*s, dtype=torch.bfloat16) * 0.05)(B, S, D)),
            "wqkv": _w((lambda *s: torch.randn(*s, dtype=torch.bfloat16) * 0.2)(D, 3 * D)),
            "wo": _w((lambda *s: torch.randn(*s, dtype=torch.bfloat16) * 0.1)(D, D)),
        }
    _A_in = _A_setup()

    def _B_setup():
        B, S, D, Dff = 1, 2048, 512, 1792
        return {
            "x": _w((lambda *s: torch.randn(*s, dtype=torch.bfloat16) * 0.05)(B, S, D)),
            "w1": _w((lambda *s: torch.randn(*s, dtype=torch.bfloat16) * 0.2)(D, Dff)),
            "w2": _w((lambda *s: torch.randn(*s, dtype=torch.bfloat16) * 0.2)(Dff, D)),
        }
    _B_in = _B_setup()

    return {
        "A_x": _A_in["x"],
        "A_wqkv": _A_in["wqkv"],
        "A_wo": _A_in["wo"],
        "B_x": _B_in["x"],
        "B_w1": _B_in["w1"],
        "B_w2": _B_in["w2"],
    }

def reference(A_x, A_wqkv, A_wo, B_x, B_w1, B_w2):
    def _A_ref(x, wqkv, wo):
        B, S, D = x.shape
        H = 32
        Dh = D // H
        qkv = (lambda x, *d: x.permute(*d))((x @ wqkv).reshape(B, S, 3, H, Dh), 2, 0, 3, 1, 4)
        q, k, v = qkv[0], qkv[1], qkv[2]
        scores = torch.einsum("bhqd,bhkd->bhqk", q, k) * (Dh ** -0.5)
        p = _softmax(scores, axis=-1)
        o = torch.einsum("bhqk,bhkd->bhqd", p, v)
        return (lambda x, *d: x.permute(*d))(o, 0, 2, 1, 3).reshape(B, S, D) @ wo

    def _B_ref(x, w1, w2):
        h = torch.einsum("bsd,df->bsf", x, w1)
        zero = torch.zeros_like(h)
        h = torch.maximum(h, zero)
        return torch.einsum("bsf,fd->bsd", h, w2)

    x = A_x
    xn = _layernorm(x, axis=-1)
    return x + _A_ref(xn, A_wqkv, A_wo) + _B_ref(xn, B_w1, B_w2)
```

```cpp
#include <hip/hip_runtime.h>
#include <hip/hip_bf16.h>
#include <stdint.h>

typedef __attribute__((ext_vector_type(8))) __bf16 bf16x8;
typedef __attribute__((ext_vector_type(4))) __bf16 bf16x4;
typedef __attribute__((ext_vector_type(16))) float f32x16;
typedef __attribute__((ext_vector_type(8))) float f32x8;
typedef __attribute__((ext_vector_type(4))) float f32x4;
typedef __attribute__((ext_vector_type(2))) float f32x2;
typedef __attribute__((ext_vector_type(4))) unsigned u32x4;
typedef __attribute__((ext_vector_type(2))) unsigned u32x2;
#define LDS_AS __attribute__((address_space(3)))
#define DI __device__ __forceinline__
#define MFMA(a, b, c) __builtin_amdgcn_mfma_f32_32x32x16_bf16((a), (b), (c), 0, 0, 0)

constexpr int S_ = 2048, D_ = 512, FF_ = 1792, NH_ = 32;
constexpr int KVTILE = 2112;
constexpr int VBLK = 544;
constexpr int KVHEAD = 136 * 1024;
constexpr size_t WS_QF = 0;
constexpr size_t WS_KV = WS_QF + (size_t)2 * 1024 * 1024;
constexpr size_t WS_XN = WS_KV + (size_t)NH_ * KVHEAD;
constexpr size_t WS_W1 = WS_XN + (size_t)2 * 1024 * 1024;
constexpr size_t WS_W2 = WS_W1 + (size_t)1792 * 1024;
constexpr size_t WS_WO = WS_W2 + (size_t)1792 * 1024;
constexpr size_t WS_HF = WS_WO + (size_t)512 * 1024;
constexpr size_t WS_OF = WS_HF + (size_t)64 * 112 * 1024;
constexpr size_t WS_Y = WS_OF + (size_t)2 * 1024 * 1024;
constexpr size_t WS_FLAG = WS_Y + (size_t)4 * 1024 * 1024;
constexpr size_t WS_END = WS_FLAG + 4096;

#define STAMPT(ws, kid, i)
constexpr float QSCALE = 0.25f * 1.4426950408889634f;

DI unsigned lds_addr(const void* p) { return (unsigned)(uintptr_t)(LDS_AS const char*)p; }
DI void glds16(const void* sbase, unsigned voff, unsigned ldst) {
  unsigned keep;
  asm volatile("s_mov_b32 %0, m0\n\ts_mov_b32 m0, %3\n\ts_nop 0\n\tglobal_load_lds_dwordx4 %1, %2\n\ts_mov_b32 m0, %0"
               : "=&s"(keep) : "v"(voff), "s"(sbase), "s"(ldst) : "memory");
}
#define WAIT_VM(n) asm volatile("s_waitcnt vmcnt(" #n ")" ::: "memory")
#define WAIT_LGKM0() asm volatile("s_waitcnt lgkmcnt(0)" ::: "memory")
#define BARRIER() __builtin_amdgcn_s_barrier()
#define TBAR(acc) __builtin_amdgcn_s_barrier()
#define TVM(acc, stmt) stmt
#define TBAR_OUT(ws, acc, i)
template <int N> DI void wait_vm() { asm volatile("s_waitcnt vmcnt(%0)" ::"i"(N) : "memory"); }

#define X_STAUX 16
DI __amdgpu_buffer_rsrc_t ws_rsrc(const void* ws) { return __builtin_amdgcn_make_buffer_rsrc((void*)ws, 0, (int)WS_END, 0x00020000); }
template <typename T> DI void st16(__amdgpu_buffer_rsrc_t rs, unsigned off, T v) {
  __builtin_amdgcn_raw_buffer_store_b128(__builtin_bit_cast(u32x4, v), rs, off, 0, X_STAUX);
}
DI bf16x8 lds_b128(const char* sm, unsigned off) { return *(const bf16x8*)(sm + off); }
DI bf16x4 lds_tr(const char* sm, unsigned off) {
  return __builtin_amdgcn_ds_read_tr16_b64_v4bf16((LDS_AS bf16x4*)(LDS_AS char*)(sm + off));
}
DI bf16x8 cat8(bf16x4 lo, bf16x4 hi) { return __builtin_shufflevector(lo, hi, 0, 1, 2, 3, 4, 5, 6, 7); }
DI bf16x8 pack_lo(const f32x16& v) { return __builtin_convertvector(__builtin_shufflevector(v, v, 0, 1, 2, 3, 4, 5, 6, 7), bf16x8); }
DI bf16x8 pack_hi(const f32x16& v) { return __builtin_convertvector(__builtin_shufflevector(v, v, 8, 9, 10, 11, 12, 13, 14, 15), bf16x8); }
DI float bf_lo(unsigned u) { return __uint_as_float(u << 16); }
DI float bf_hi(unsigned u) { return __uint_as_float(u & 0xffff0000u); }
DI int vslot(int d, int h) {
  int s = (d < 4) ? d : (d >= 12) ? d - 8 : (d < 11) ? d + 4 : -1;
  return (s >= 0) ? (16 * h + s) : (32 + h);
}

constexpr int K1_SLOT = 28672, K1_WOFF = 16384, K1_NSLOT = 5;
constexpr int K1_STATS = K1_SLOT * K1_NSLOT;
constexpr int K1_WPART = K1_STATS + 1024;
constexpr int K1_LDS = K1_WPART + 1536;

template <int NV>
DI void k1_gemm_wave(char* sm, int w, int lane, int rg, int cg, char* ws) {
  const int h = lane >> 5, r31 = lane & 31;
  STAMPT(ws, 1, 0);
  unsigned aoff[4];
  {
    const int row = 32 * w + r31;
#pragma unroll
    for (int ks = 0; ks < 4; ++ks) aoff[ks] = row * 128 + (((2 * ks + h) ^ ((row >> 1) & 7)) << 4);
  }
  const unsigned boff = K1_WOFF + (8 * h + ((lane & 15) >> 2)) * 192 + (16 * ((lane >> 4) & 1) + 4 * (lane & 3)) * 2;
  f32x16 acc[3] = {};
  bf16x8 fa[4], fb[4][3];
#pragma unroll
  for (int i = 2; i < 4; ++i) {
    fa[i] = bf16x8{};
#pragma unroll
    for (int j = 0; j < 3; ++j) fb[i][j] = bf16x8{};
  }
#define K1_MMA(ps)                                                        \
  _Pragma("unroll") for (int j = 0; j < 3; ++j) {                         \
    if (j >= 3 - NV) acc[j] = MFMA(fa[ps], fb[ps][j], acc[j]);   \
    else acc[j] = MFMA(fb[ps][j], fa[ps], acc[j]);               \
  }
  int slot = 0;
  unsigned long long bw = 0;
#pragma nounroll
  for (int s = 0; s < 8; ++s) {
    WAIT_LGKM0();
    if (s == 1) bw = 0;
    TBAR(bw);
    if (s == 0) STAMPT(ws, 1, 1);
    if (s == 1) STAMPT(ws, 1, 2);
    if (s == 4) STAMPT(ws, 1, 3);
    if (s == 7) STAMPT(ws, 1, 4);
    const char* sl = sm + slot;
#pragma unroll
    for (int ks = 0; ks < 4; ++ks) {
      fa[ks] = lds_b128(sl, aoff[ks]);
#pragma unroll
      for (int j = 0; j < 3; ++j)
        fb[ks][j] = cat8(lds_tr(sl, boff + ks * 16 * 192 + 64 * j), lds_tr(sl, boff + (ks * 16 + 4) * 192 + 64 * j));
      __builtin_amdgcn_sched_barrier(0);
      K1_MMA((ks + 2) & 3);
      __builtin_amdgcn_sched_barrier(0);
    }
    slot += K1_SLOT;
    if (slot == K1_NSLOT * K1_SLOT) slot = 0;
  }
  K1_MMA(2);
  K1_MMA(3);
  TBAR_OUT(ws, bw, 1);
  STAMPT(ws, 1, 5);
  WAIT_LGKM0();
  BARRIER();
  STAMPT(ws, 1, 6);
  const int rt = rg * 4 + w;
  const __amdgpu_buffer_rsrc_t rs = ws_rsrc(ws);
  const f32x2* stats = (const f32x2*)(sm + K1_STATS) + 32 * w;
  const float* wsum = (const float*)(sm + K1_WPART);
#pragma unroll
  for (int j = 0; j < 3; ++j) {
    const int ct = cg * 3 + j, role = ct >> 4, hp = (ct & 15) * 2;
    f32x16 v;
    if (j >= 3 - NV) {
      const float wsn = wsum[32 * j + r31];
#pragma unroll
      for (int g = 0; g < 4; ++g) {
        f32x4 s0 = *(const f32x4*)(stats + 8 * g + 4 * h), s1 = *(const f32x4*)(stats + 8 * g + 4 * h + 2);
        v[4 * g + 0] = acc[j][4 * g + 0] * s0[0] + s0[1] * wsn;
        v[4 * g + 1] = acc[j][4 * g + 1] * s0[2] + s0[3] * wsn;
        v[4 * g + 2] = acc[j][4 * g + 2] * s1[0] + s1[1] * wsn;
        v[4 * g + 3] = acc[j][4 * g + 3] * s1[2] + s1[3] * wsn;
      }
      const unsigned base = (unsigned)WS_KV + (hp + (r31 >> 4)) * KVHEAD + rt * KVTILE + 1024;
      const int sl = vslot(r31 & 15, h);
      st16(rs, base + sl * 16, pack_lo(v));
      st16(rs, base + VBLK + sl * 16, pack_hi(v));
      if ((lane & 15) == 0)
        st16(rs, base + h * VBLK + 15 * 16, u32x4{0x3f803f80u, 0x3f803f80u, 0x3f803f80u, 0x3f803f80u});
    } else {
      f32x2 st = stats[r31];
      if (role == 0) { st[0] *= QSCALE; st[1] *= QSCALE; }
#pragma unroll
      for (int g = 0; g < 4; ++g) {
        f32x4 wsv = *(const f32x4*)(wsum + 32 * j + 8 * g + 4 * h);
#pragma unroll
        for (int e = 0; e < 4; ++e) v[4 * g + e] = acc[j][4 * g + e] * st[0] + st[1] * wsv[e];
      }
      if (role == 0) {
        const unsigned base = (unsigned)WS_QF + (hp * 64 + rt) * 1024 + lane * 16;
        st16(rs, base, pack_lo(v));
        st16(rs, base + 64 * 1024, pack_hi(v));
      } else {
        const unsigned base = (unsigned)WS_KV + hp * KVHEAD + rt * KVTILE + lane * 16;
        st16(rs, base, pack_lo(v));
        st16(rs, base + KVHEAD, pack_hi(v));
      }
    }
  }
  STAMPT(ws, 1, 7);
}

constexpr int K1_SIDE = 3 * K1_SLOT;

DI void k1_stats_wave(char* sm, int hw, int lane, int rg, int cg, char* ws) {
  STAMPT(ws, 1, 0);
  const int t = hw * 64 + lane, row = t >> 2, qt = t & 3;
  unsigned soff[2];
#pragma unroll
  for (int i = 0; i < 2; ++i) soff[i] = row * 128 + qt * 32 + ((i ^ ((row >> 1) & 1)) << 4);
  float sum4[4] = {}, sq4[4] = {}, shift = 0.f;
  const unsigned boff = K1_WOFF + (8 * (lane >> 5) + ((lane & 15) >> 2)) * 192 + (16 * ((lane >> 4) & 1) + 4 * (lane & 3)) * 2 + 64 * hw;
  const bf16x8 ones = __builtin_bit_cast(bf16x8, u32x4{0x3f803f80u, 0x3f803f80u, 0x3f803f80u, 0x3f803f80u});
  f32x16 wacc = {};
  int slot = 0;
  unsigned long long bw = 0;
  u32x4 ud[2][2] = {};
  bf16x8 wb[2][4] = {};
  const bool dow = hw < 3;
  auto math = [&](const u32x4 (&up)[2], const bf16x8 (&wbp)[4]) {
#pragma unroll
    for (int i = 0; i < 2; ++i) {
#pragma unroll
      for (int e = 0; e < 4; ++e) {
        float a = bf_lo(up[i][e]) - shift, b = bf_hi(up[i][e]) - shift;
        asm volatile("" : "+v"(a), "+v"(b));
        sum4[e & 1] += a; sq4[e & 1] = fmaf(a, a, sq4[e & 1]);
        asm volatile("" : "+v"(sum4[e & 1]), "+v"(sq4[e & 1]));
        sum4[2 + (e & 1)] += b; sq4[2 + (e & 1)] = fmaf(b, b, sq4[2 + (e & 1)]);
        asm volatile("" : "+v"(sum4[2 + (e & 1)]), "+v"(sq4[2 + (e & 1)]));
      }
    }
    if (dow) {
#pragma unroll
      for (int ks = 0; ks < 4; ++ks) wacc = MFMA(ones, wbp[ks], wacc);
    }
  };
#pragma nounroll
  for (int s2 = 0; s2 < 4; ++s2) {
#pragma unroll
    for (int par = 0; par < 2; ++par) {
      if (s2 == 0 && par == 1) bw = 0;
      TBAR(bw);
      const char* sl = sm + slot;
#pragma unroll
      for (int i = 0; i < 2; ++i) ud[par][i] = *(const u32x4*)(sl + soff[i]);
      if (dow) {
#pragma unroll
        for (int ks = 0; ks < 4; ++ks) wb[par][ks] = cat8(lds_tr(sl, boff + ks * 16 * 192), lds_tr(sl, boff + (ks * 16 + 4) * 192));
      }
      __builtin_amdgcn_sched_barrier(0);
      if (par == 0 && s2 == 0) {
        shift = bf_lo(*(const unsigned*)(sl + row * 128));
      } else {
        math(ud[par ^ 1], wb[par ^ 1]);
      }
      __builtin_amdgcn_sched_barrier(0);
      WAIT_LGKM0();
      slot += K1_SLOT;
      if (slot == K1_NSLOT * K1_SLOT) slot = 0;
    }
  }
  math(ud[1], wb[1]);
  TBAR_OUT(ws, bw, 1);
  STAMPT(ws, 1, 5);
  if (lane < 32 && hw < 3) *(float*)(sm + K1_WPART + (32 * hw + lane) * 4) = wacc[0];
  float sum = (sum4[0] + sum4[1]) + (sum4[2] + sum4[3]), sq = (sq4[0] + sq4[1]) + (sq4[2] + sq4[3]);
  sum += __shfl_xor(sum, 1); sq += __shfl_xor(sq, 1);
  sum += __shfl_xor(sum, 2); sq += __shfl_xor(sq, 2);
  {
    float ms = sum * (1.f / 512.f);
    float var = fmaxf(sq * (1.f / 512.f) - ms * ms, 0.f);
    float rstd = 1.0f / sqrtf(var + 1e-5f);
    float mean = ms + shift;
    if (qt == 0) *(f32x2*)(sm + K1_STATS + row * 8) = f32x2{rstd, -mean * rstd};
  }
  WAIT_LGKM0();
  BARRIER();
  STAMPT(ws, 1, 6);
  {
    const int p = lane & 7, lrow = cg * 8 + p;
    const f32x2 st = *(const f32x2*)(sm + K1_STATS + lrow * 8);
    const int grow = rg * 128 + lrow;
    const int c = (lane >> 3) + 8 * hw;
    u32x4 u = *(const u32x4*)(sm + K1_SIDE + 16384 + p * 1024 + c * 16);
    f32x8 v;
#pragma unroll
    for (int e = 0; e < 4; ++e) { v[2 * e] = bf_lo(u[e]) * st[0] + st[1]; v[2 * e + 1] = bf_hi(u[e]) * st[0] + st[1]; }
    st16(ws_rsrc(ws), (unsigned)WS_XN + (((grow >> 5) * 32 + (c >> 1)) * 64 + (c & 1) * 32 + (grow & 31)) * 16,
         __builtin_convertvector(v, bf16x8));
  }
  STAMPT(ws, 1, 7);
}

DI void k1_loader_wave(char* sm, unsigned sbase, const char* xg, const char* wqkv, const char* w1, const char* w2,
                       const char* wo, int lw, int lane, int bid, int rg, int cg, int rot, char* ws) {
  unsigned xoff[4], woff[3];
#pragma unroll
  for (int p = 0; p < 4; ++p) {
    int c = (lane & 7) ^ ((4 * p + (lane >> 4)) & 7);
    xoff[p] = (unsigned)((rg * 128 + 32 * lw + 8 * p + (lane >> 3)) * 1024 + c * 16);
  }
#pragma unroll
  for (int i = 0; i < 3; ++i) {
    int idx = 64 * (3 * lw + i) + lane;
    int row = idx / 12, ch = idx - row * 12;
    woff[i] = (unsigned)(row * 3072 + cg * 192 + ch * 16);
  }
  auto issue = [&](int s, int slot) {
    const int ks = (s + rot) & 7;
    const unsigned dst = sbase + slot;
#pragma unroll
    for (int i = 0; i < 4; ++i) glds16(xg + ks * 128, xoff[i], dst + lw * 4096 + i * 1024);
#pragma unroll
    for (int i = 0; i < 3; ++i) glds16(wqkv + (size_t)ks * 64 * 3072, woff[i], dst + K1_WOFF + lw * 3072 + i * 1024);
  };
  issue(0, 0);
  issue(1, K1_SLOT);
  const char* swsrc; int sld, kb, nb, sks; size_t sdst; bool perm;
  if (bid < 112) { swsrc = w1; sld = FF_ * 2; kb = bid / 14; nb = bid - kb * 14; sks = 32; sdst = WS_W1; perm = false; }
  else if (bid < 224) { int v = bid - 112; swsrc = w2; sld = D_ * 2; kb = v >> 2; nb = v & 3; sks = 112; sdst = WS_W2; perm = true; }
  else { int v = bid - 224; swsrc = wo; sld = D_ * 2; kb = v >> 2; nb = v & 3; sks = 32; sdst = WS_WO; perm = true; }
  const int h = lane >> 5, q = (lane & 15) >> 2;
  const unsigned cb = (16 * ((lane >> 4) & 1) + 4 * (lane & 3)) * 2;
  int islot = 3 * K1_SLOT;
  unsigned long long bw = 0, vw = 0;
#pragma nounroll
  for (int s = 0; s < 8; ++s) {
    if (s == 1) { bw = 0; vw = 0; }
    TVM(vw, if (s == 0) wait_vm<7>(); else if (s <= 5) wait_vm<14>(); else if (s == 6) wait_vm<13>(); else wait_vm<6>());
    WAIT_LGKM0();
    TBAR(bw);
    if (s <= 4) {
      if (s == 0) issue(2, 2 * K1_SLOT);
      issue(s + 3, islot);
      islot += K1_SLOT;
      if (islot == K1_NSLOT * K1_SLOT) islot = 0;
    } else if (s == 5) {
#pragma unroll
      for (int i = 0; i < 4; ++i) {
        int p = 4 * lw + i;
        glds16(swsrc + (size_t)(kb * 64 + 4 * p) * sld + nb * 256, (unsigned)((lane >> 4) * sld + (lane & 15) * 16),
               sbase + K1_SIDE + p * 1024);
      }
#pragma unroll
      for (int i = 0; i < 2; ++i) {
        int p = 2 * lw + i;
        glds16(xg + (size_t)(rg * 128 + cg * 8 + p) * 1024, (unsigned)(lane * 16), sbase + K1_SIDE + 16384 + p * 1024);
      }
    }
  }
  TBAR_OUT(ws, bw, 1);
  TBAR_OUT(ws, vw, 2);
  WAIT_VM(0);
  WAIT_LGKM0();
  BARRIER();
  {
    const int r0 = perm ? 4 * h : 8 * h, r1 = perm ? 8 + 4 * h : 8 * h + 4;
    const unsigned o0 = K1_SIDE + (16 * lw + r0 + q) * 256 + cb, o1 = K1_SIDE + (16 * lw + r1 + q) * 256 + cb;
#pragma unroll
    for (int nt = 0; nt < 4; ++nt) {
      bf16x8 f = cat8(lds_tr(sm, o0 + nt * 64), lds_tr(sm, o1 + nt * 64));
      st16(ws_rsrc(ws), (unsigned)sdst + ((nb * 4 + nt) * sks + kb * 4 + lw) * 1024 + lane * 16, f);
    }
  }
}

__global__ __launch_bounds__(1024) void k1_qkv(const char* __restrict__ x, const char* __restrict__ wqkv,
                                              const char* __restrict__ w1, const char* __restrict__ w2,
                                              const char* __restrict__ wo, char* __restrict__ ws) {
  extern __shared__ __attribute__((aligned(16))) char sm[];
  const int lane = threadIdx.x & 63, wave = __builtin_amdgcn_readfirstlane(threadIdx.x >> 6);
  const int bid = blockIdx.x, xcd = bid & 7, idx = bid >> 3;
  const int rg = 4 * (xcd >> 1) + (idx & 3), cg = 8 * (xcd & 1) + (idx >> 2);
  if (wave < 4) {
    const int rot = ((idx >> 2) + 2 * (idx & 3)) & 7;
    k1_loader_wave(sm, lds_addr(sm), x, wqkv, w1, w2, wo, wave, lane, bid, rg, cg, rot, ws);
  } else if (wave < 8) {
    if (cg < 10) k1_gemm_wave<0>(sm, wave - 4, lane, rg, cg, ws);
    else if (cg == 10) k1_gemm_wave<1>(sm, wave - 4, lane, rg, cg, ws);
    else k1_gemm_wave<3>(sm, wave - 4, lane, rg, cg, ws);
  } else {
    if (bid == 0 && wave < 12) st16(ws_rsrc(ws), (unsigned)WS_FLAG + (wave - 8) * 1024 + lane * 16, u32x4{0u, 0u, 0u, 0u});
    k1_stats_wave(sm, wave - 8, lane, rg, cg, ws);
  }
}

__global__ __launch_bounds__(256) void k3_out(const char* __restrict__ ws, char* __restrict__ out) {
  __shared__ __attribute__((aligned(16))) f32x4 red[4][16][64];
  const int lane = threadIdx.x & 63, wave = __builtin_amdgcn_readfirstlane(threadIdx.x >> 6);
  const int bid = blockIdx.x, xcd = bid & 7, idx = bid >> 3;
  const int rp = xcd * 4 + (idx & 3), np = idx >> 2;
  const bf16x8* A0 = (const bf16x8*)(ws + WS_WO) + ((size_t)(np * 2) * 32 + wave * 8) * 64 + lane;
  const bf16x8* B0 = (const bf16x8*)(ws + WS_OF) + ((size_t)(rp * 2) * 32 + wave * 8) * 64 + lane;
  const int i = wave >> 1, j = wave & 1, rt = rp * 2 + j, nt = np * 2 + i;
  f32x4 yv[4];
#pragma unroll
  for (int g = 0; g < 4; ++g) yv[g] = *((const f32x4*)(ws + WS_Y) + ((size_t)(rt * 16 + nt) * 4 + g) * 64 + lane);
  f32x16 acc[4] = {};
  {
    bf16x8 a0[8], a1[8], b0[8], b1[8];
#pragma unroll
    for (int ks = 0; ks < 8; ++ks) { b0[ks] = B0[ks * 64]; b1[ks] = B0[(32 + ks) * 64]; a0[ks] = A0[ks * 64]; a1[ks] = A0[(32 + ks) * 64]; }
    __builtin_amdgcn_sched_barrier(0);
#pragma unroll
    for (int ks = 0; ks < 8; ++ks) {
      acc[0] = MFMA(a0[ks], b0[ks], acc[0]);
      acc[1] = MFMA(a0[ks], b1[ks], acc[1]);
      acc[2] = MFMA(a1[ks], b0[ks], acc[2]);
      acc[3] = MFMA(a1[ks], b1[ks], acc[3]);
    }
  }
#pragma unroll
  for (int t = 0; t < 4; ++t)
#pragma unroll
    for (int g = 0; g < 4; ++g)
      red[wave][t * 4 + g][lane] = f32x4{acc[t][4 * g], acc[t][4 * g + 1], acc[t][4 * g + 2], acc[t][4 * g + 3]};
  __syncthreads();
  const int h = lane >> 5, r31 = lane & 31;
#pragma unroll
  for (int g = 0; g < 4; ++g) {
    f32x4 v = yv[g];
#pragma unroll
    for (int p = 0; p < 4; ++p) v += red[p][wave * 4 + g][lane];
    bf16x4 o = __builtin_convertvector(v, bf16x4);
    *(bf16x4*)(out + ((size_t)(rt * 32 + r31) * D_ + nt * 32 + 8 * g + 4 * h) * 2) = o;
  }
}

constexpr int K2_RED = KVHEAD;
constexpr int K2_FLG = K2_RED + 16384;
constexpr int K2_LDS = K2_FLG + 64;
constexpr float SM_DELTA = 60.f;
typedef __attribute__((address_space(1))) unsigned gu32;
#define X_AUXB 16
#define SB() __builtin_amdgcn_sched_barrier(0)

DI float xhalf_max(float v) {
  auto r = __builtin_amdgcn_permlane32_swap(__float_as_uint(v), __float_as_uint(v), false, false);
  return fmaxf(__uint_as_float(r[0]), __uint_as_float(r[1]));
}
DI float max16(const f32x16& s) {
  float m01 = fmaxf(fmaxf(s[0], s[1]), s[2]), m23 = fmaxf(fmaxf(s[3], s[4]), s[5]);
  float m45 = fmaxf(fmaxf(s[6], s[7]), s[8]), m67 = fmaxf(fmaxf(s[9], s[10]), s[11]);
  float m89 = fmaxf(fmaxf(s[12], s[13]), s[14]);
  return fmaxf(fmaxf(fmaxf(m01, m23), m45), fmaxf(fmaxf(m67, m89), s[15]));
}
DI void lds_signal(char* sm, int word, int lane) {
  if (lane == 0) __hip_atomic_fetch_add((LDS_AS unsigned*)(LDS_AS char*)(sm + K2_FLG) + word, 1u, __ATOMIC_RELAXED, __HIP_MEMORY_SCOPE_WORKGROUP);
  asm volatile("" ::: "memory");
}
DI void lds_wait(char* sm, int word, unsigned target) {
  while (true) {
    unsigned v = __hip_atomic_load((LDS_AS unsigned*)(LDS_AS char*)(sm + K2_FLG) + word, __ATOMIC_RELAXED, __HIP_MEMORY_SCOPE_WORKGROUP);
    if ((unsigned)__builtin_amdgcn_readfirstlane(v) >= target) break;
    __builtin_amdgcn_s_sleep(1);
  }
  asm volatile("" ::: "memory");
}

template <int NKS, int PF, int AUXB>
DI void dgemm64_ring(__amdgpu_buffer_rsrc_t rs, unsigned a0, unsigned a1, unsigned b0, unsigned b1, int rot, f32x16 (&acc)[4]) {
  u32x4 A0[PF], A1[PF], B0[PF], B1[PF];
  int ko = rot * 1024;
#pragma unroll
  for (int kk = 0; kk < NKS + PF - 1; ++kk) {
    if (kk < NKS) {
      const int sl = kk % PF;
      A0[sl] = __builtin_amdgcn_raw_buffer_load_b128(rs, a0, ko, 0);
      B0[sl] = __builtin_amdgcn_raw_buffer_load_b128(rs, b0, ko, AUXB);
      A1[sl] = __builtin_amdgcn_raw_buffer_load_b128(rs, a1, ko, 0);
      B1[sl] = __builtin_amdgcn_raw_buffer_load_b128(rs, b1, ko, AUXB);
      ko += 1024;
      if (ko == NKS * 1024) ko = 0;
    }
    SB();
    if (kk >= PF - 1) {
      const int sl = (kk - (PF - 1)) % PF;
      const bf16x8 fa0 = __builtin_bit_cast(bf16x8, A0[sl]), fa1 = __builtin_bit_cast(bf16x8, A1[sl]);
      const bf16x8 fb0 = __builtin_bit_cast(bf16x8, B0[sl]), fb1 = __builtin_bit_cast(bf16x8, B1[sl]);
      acc[0] = MFMA(fa0, fb0, acc[0]);
      acc[2] = MFMA(fa1, fb0, acc[2]);
      acc[1] = MFMA(fa0, fb1, acc[1]);
      acc[3] = MFMA(fa1, fb1, acc[3]);
    }
    SB();
  }
}

DI void k2_ffn_wave(char* sm, const char* __restrict__ x, char* __restrict__ ws, int w, int lane, int panel, int ci, int head) {
  {
    const unsigned sb = lds_addr(sm) + w * 1024, dl = lane * 16;
    const char* kvg = ws + WS_KV + (size_t)head * KVHEAD + w * 1024;
#pragma unroll
    for (int i = 0; i < 8; ++i) glds16(kvg + i * 4096, dl, sb + i * 4096);
    WAIT_LGKM0();
    BARRIER();
    WAIT_VM(4);
    lds_signal(sm, 0, lane);
#pragma unroll
    for (int i = 8; i < 34; ++i) glds16(kvg + i * 4096, dl, sb + i * 4096);
    WAIT_VM(22);
    lds_signal(sm, 1, lane);
    WAIT_VM(0);
    lds_signal(sm, 2, lane);
  }
#define X_FPRIO 2
  __builtin_amdgcn_s_setprio(X_FPRIO);
  const __amdgpu_buffer_rsrc_t rs = __builtin_amdgcn_make_buffer_rsrc((void*)ws, 0, (int)WS_END, 0x00020000);
  gu32* flags = (gu32*)(ws + WS_FLAG) + panel * 128;
  STAMPT(ws, 2, 0);
  if (ci < 28) {
    const int rt0 = panel * 8 + 2 * w, nt0 = 2 * ci;
    const unsigned a0 = (unsigned)WS_W1 + nt0 * 32768 + lane * 16, b0 = (unsigned)WS_XN + rt0 * 32768 + lane * 16;
    f32x16 acc[4] = {};
    dgemm64_ring<32, 5, 0>(rs, a0, a0 + 32768, b0, b0 + 32768, (8 * w + ci) & 31, acc);
#pragma unroll
    for (int i = 0; i < 2; ++i)
#pragma unroll
      for (int j = 0; j < 2; ++j) {
        f32x16 v = acc[2 * i + j];
#pragma unroll
        for (int e = 0; e < 16; ++e) v[e] = fmaxf(v[e], 0.f);
        const unsigned dst = (unsigned)WS_HF + ((rt0 + j) * 112 + (nt0 + i) * 2) * 1024 + lane * 16;
        __builtin_amdgcn_raw_buffer_store_b128(__builtin_bit_cast(u32x4, pack_lo(v)), rs, dst, 0, 16);
        __builtin_amdgcn_raw_buffer_store_b128(__builtin_bit_cast(u32x4, pack_hi(v)), rs, dst + 1024, 0, 16);
      }
    WAIT_VM(0);
    if (lane == 0) __hip_atomic_store(flags + w * 32 + ci, 1u, __ATOMIC_RELAXED, __HIP_MEMORY_SCOPE_AGENT);
  }
  STAMPT(ws, 2, 1);
  const int rp2 = ci & 3, np2 = ci >> 2;
  {
    gu32* f = flags + rp2 * 32 + 7 * w + min(lane, 6);
    for (unsigned spins = 0; spins < (1u << 20); ++spins) {
      unsigned v = __hip_atomic_load(f, __ATOMIC_RELAXED, __HIP_MEMORY_SCOPE_AGENT);
      if (__all(v == 1u)) break;
      __builtin_amdgcn_s_sleep(16);
    }
    __builtin_amdgcn_fence(__ATOMIC_ACQUIRE, "wavefront");
    asm volatile("" ::: "memory");
  }
  STAMPT(ws, 2, 2);
  const int rt0 = panel * 8 + 2 * rp2, nt0 = 2 * np2;
  f32x16 acc[4] = {};
  {
    const unsigned a0 = (unsigned)WS_W2 + (nt0 * 112 + 28 * w) * 1024 + lane * 16;
    const unsigned b0 = (unsigned)WS_HF + (rt0 * 112 + 28 * w) * 1024 + lane * 16;
    dgemm64_ring<28, 5, X_AUXB>(rs, a0, a0 + 112 * 1024, b0, b0 + 112 * 1024, (7 * rp2 + 3 * np2) % 28, acc);
  }
  STAMPT(ws, 2, 3);
  f32x4* red = (f32x4*)(sm + K2_RED) + lane;
  if (w < 3) {
    lds_wait(sm, 4 + w, 1);
#pragma unroll
    for (int t = 0; t < 4; ++t)
#pragma unroll
      for (int g = 0; g < 4; ++g) {
        f32x4 r = red[(t * 4 + g) * 64];
#pragma unroll
        for (int e = 0; e < 4; ++e) acc[t][4 * g + e] += r[e];
      }
  }
  if (w > 0) {
#pragma unroll
    for (int t = 0; t < 4; ++t)
#pragma unroll
      for (int g = 0; g < 4; ++g)
        red[(t * 4 + g) * 64] = f32x4{acc[t][4 * g], acc[t][4 * g + 1], acc[t][4 * g + 2], acc[t][4 * g + 3]};
    WAIT_LGKM0();
    lds_signal(sm, 4 + w - 1, lane);
  } else {
    const int h = lane >> 5, r31 = lane & 31;
#pragma unroll
    for (int t = 0; t < 4; ++t) {
      const int rt = rt0 + (t & 1), nt = nt0 + (t >> 1);
#pragma unroll
      for (int g = 0; g < 4; ++g) {
        f32x4 v = f32x4{acc[t][4 * g], acc[t][4 * g + 1], acc[t][4 * g + 2], acc[t][4 * g + 3]};
        u32x2 xv = *(const u32x2*)(x + ((size_t)(rt * 32 + r31) * D_ + nt * 32 + 8 * g + 4 * h) * 2);
        v[0] += bf_lo(xv[0]); v[1] += bf_hi(xv[0]); v[2] += bf_lo(xv[1]); v[3] += bf_hi(xv[1]);
        st16(rs, (unsigned)WS_Y + ((rt * 16 + nt) * 4 + g) * 1024 + lane * 16, v);
      }
    }
  }
  STAMPT(ws, 2, 4);
}

__global__ __launch_bounds__(768) void k2_attn(const char* __restrict__ x, char* __restrict__ ws) {
  extern __shared__ __attribute__((aligned(16))) char sm[];
  const int lane = threadIdx.x & 63, wave = __builtin_amdgcn_readfirstlane(threadIdx.x >> 6);
  const int bid = blockIdx.x, xcd = bid & 7, idx = bid >> 3;
  if (threadIdx.x < 16) *(unsigned*)(sm + K2_FLG + threadIdx.x * 4) = 0u;
  const int head = xcd * 4 + (idx >> 3), qb = idx & 7, rt = qb * 8 + wave;
  if (wave >= 8) {
    k2_ffn_wave(sm, x, ws, wave - 8, lane, xcd, idx, head);
    return;
  }
  STAMPT(ws, 2, 0);
  const bf16x8 qf = *((const bf16x8*)(ws + WS_QF) + ((size_t)(head * 64 + rt) * 64 + lane));
  const int h = lane >> 5, r31 = lane & 31;
  const unsigned koff = lane * 16;
  const unsigned voff = 1024 + ((r31 < 16) ? vslot(r31, h) : 15) * 16;
  WAIT_LGKM0();
  BARRIER();
  f32x16 negm = {}, O = {};
#define EXP(v, i) v[i] = __builtin_amdgcn_exp2f(v[i])
#define TILE_STEP(SC, SN, OFFK2, OFFV0N, OFFV1)                                   \
  {                                                                               \
    SN = MFMA(kn, qf, negm);                                                      \
    SB();                                                                         \
    kn = lds_b128(kp, OFFK2);                                                     \
    EXP(SC, 0); EXP(SC, 1); EXP(SC, 2); EXP(SC, 3); EXP(SC, 4); EXP(SC, 5);       \
    SB();                                                                         \
    O = MFMA(v1p, p1p, O);                                                        \
    SB();                                                                         \
    v1p = lds_b128(vp, OFFV1);                                                    \
    EXP(SC, 6); EXP(SC, 7);                                                       \
    bf16x8 p0 = pack_lo(SC);                                                      \
    SB();                                                                         \
    EXP(SC, 8); EXP(SC, 9);                                                       \
    SB();                                                                         \
    O = MFMA(v0, p0, O);                                                          \
    SB();                                                                         \
    v0 = lds_b128(vp, OFFV0N);                                                    \
    EXP(SC, 10); EXP(SC, 11); EXP(SC, 12); EXP(SC, 13); EXP(SC, 14); EXP(SC, 15); \
    p1p = pack_hi(SC);                                                            \
    SB();                                                                         \
  }
#define TILE4()                                                        \
  TILE_STEP(sA, sB, 2 * KVTILE, 1 * KVTILE, 0 * KVTILE + VBLK);        \
  TILE_STEP(sB, sA, 3 * KVTILE, 2 * KVTILE, 1 * KVTILE + VBLK);        \
  TILE_STEP(sA, sB, 4 * KVTILE, 3 * KVTILE, 2 * KVTILE + VBLK);        \
  TILE_STEP(sB, sA, 5 * KVTILE, 4 * KVTILE, 3 * KVTILE + VBLK);        \
  kp += 4 * KVTILE; vp += 4 * KVTILE;
  {
    const char* kp = sm + koff;
    const char* vp = sm + voff;
    f32x16 sA = {}, sB;
    bf16x8 kn = {}, v0 = {}, v1p = {}, p1p = {};
#define X_SW 16
    int ev = 0;
    for (int t = 0; t < 64; t += 4) {
      if (__builtin_expect(t == ev, 0)) {
        if (t <= 0) {
          if (t == 0) { lds_wait(sm, 0, 4); STAMPT(ws, 2, 1); }
          kp = sm + koff; vp = sm + voff;
          const f32x16 z = {};
          const bf16x8 k0 = lds_b128(kp, 0);
          kn = lds_b128(kp, KVTILE);
          v0 = lds_b128(vp, 0);
          v1p = lds_b128(vp, VBLK);
          f32x16 s0 = MFMA(k0, qf, z), s1 = MFMA(kn, qf, z);
          const float nm = -(xhalf_max(fmaxf(max16(s0), max16(s1))) + SM_DELTA);
#pragma unroll
          for (int e = 0; e < 16; ++e) { negm[e] = nm; O[e] = 0.f; }
          p1p = bf16x8{};
          sA = MFMA(k0, qf, negm);
          ev = t + 4;
        } else {
          lds_wait(sm, t == 4 ? 1 : 2, 4);
          if (t != 4 && wave >= 4) __builtin_amdgcn_s_setprio(1);
          ev = (t == 4) ? X_SW : -100;
        }
      }
      TILE4();
      asm volatile("" : "+v"(O), "+v"(sA), "+v"(p1p));
    }
    O = MFMA(v1p, p1p, O);
  }
  STAMPT(ws, 2, 2);
  float l = O[8];
  bool ok = (l < 0x1p100f);
#pragma unroll
  for (int e = 0; e < 8; ++e) ok = ok && (fabsf(O[e]) < 0x1p126f);
  if (!__all(ok)) {
    float m = -INFINITY;
#pragma unroll
    for (int e = 0; e < 16; ++e) O[e] = 0.f;
    for (int t = 0; t < 64; ++t) {
      f32x16 z = {};
      f32x16 sc = MFMA(lds_b128(sm, koff + t * KVTILE), qf, z);
      bf16x8 v0 = lds_b128(sm, voff + t * KVTILE), v1 = lds_b128(sm, voff + t * KVTILE + VBLK);
      float mn = fmaxf(m, xhalf_max(max16(sc)));
      float alpha = __builtin_amdgcn_exp2f(m - mn);
      m = mn;
#pragma unroll
      for (int e = 0; e < 16; ++e) { O[e] *= alpha; sc[e] = __builtin_amdgcn_exp2f(sc[e] - mn); }
      O = MFMA(v0, pack_lo(sc), O);
      O = MFMA(v1, pack_hi(sc), O);
    }
    l = O[8];
  }
  const float rl = 1.0f / l;
#pragma unroll
  for (int e = 0; e < 8; ++e) O[e] *= rl;
  st16(ws_rsrc(ws), (unsigned)WS_OF + (rt * 32 + head) * 1024 + lane * 16, pack_lo(O));
  STAMPT(ws, 2, 3);
}

extern "C" void kernel_launch(void* const* d_in, const int* in_sizes, int n_in, void* d_out, int out_size, void* d_ws,
                              size_t ws_size, hipStream_t stream) {
  const char* x = (const char*)d_in[0];
  const char* wqkv = (const char*)d_in[1];
  const char* wo = (const char*)d_in[2];
  const char* w1 = (const char*)d_in[4];
  const char* w2 = (const char*)d_in[5];
  char* ws = (char*)d_ws;
  (void)hipFuncSetAttribute((const void*)k1_qkv, hipFuncAttributeMaxDynamicSharedMemorySize, K1_LDS);
  (void)hipFuncSetAttribute((const void*)k2_attn, hipFuncAttributeMaxDynamicSharedMemorySize, K2_LDS);
  hipLaunchKernelGGL(k1_qkv, dim3(256), dim3(1024), K1_LDS, stream, x, wqkv, w1, w2, wo, ws);
  hipLaunchKernelGGL(k2_attn, dim3(256), dim3(768), K2_LDS, stream, x, ws);
  hipLaunchKernelGGL(k3_out, dim3(256), dim3(256), 0, stream, (const char*)ws, (char*)d_out);
}
```

```cpp
#include <hip/hip_runtime.h>
#include <hip/hip_bf16.h>
#include <stdint.h>

typedef __attribute__((ext_vector_type(8))) __bf16 bf16x8;
typedef __attribute__((ext_vector_type(4))) __bf16 bf16x4;
typedef __attribute__((ext_vector_type(16))) float f32x16;
typedef __attribute__((ext_vector_type(8))) float f32x8;
typedef __attribute__((ext_vector_type(4))) float f32x4;
typedef __attribute__((ext_vector_type(2))) float f32x2;
typedef __attribute__((ext_vector_type(4))) unsigned u32x4;
typedef __attribute__((ext_vector_type(2))) unsigned u32x2;
#define LDS_AS __attribute__((address_space(3)))
#define DI __device__ __forceinline__
#define MFMA(a, b, c) __builtin_amdgcn_mfma_f32_32x32x16_bf16((a), (b), (c), 0, 0, 0)

constexpr int S_ = 2048, D_ = 512, FF_ = 1792, NH_ = 32;
constexpr int KVTILE = 2112;
constexpr int VBLK = 544;
constexpr int KVHEAD = 136 * 1024;
constexpr size_t WS_QF = 0;
constexpr size_t WS_KV = WS_QF + (size_t)2 * 1024 * 1024;
constexpr size_t WS_XN = WS_KV + (size_t)NH_ * KVHEAD;
constexpr size_t WS_W1 = WS_XN + (size_t)2 * 1024 * 1024;
constexpr size_t WS_W2 = WS_W1 + (size_t)1792 * 1024;
constexpr size_t WS_WO = WS_W2 + (size_t)1792 * 1024;
constexpr size_t WS_HF = WS_WO + (size_t)512 * 1024;
constexpr size_t WS_OF = WS_HF + (size_t)64 * 112 * 1024;
constexpr size_t WS_Y = WS_OF + (size_t)2 * 1024 * 1024;
constexpr size_t WS_FLAG = WS_Y + (size_t)4 * 1024 * 1024;
constexpr size_t WS_FLAG2 = WS_FLAG + 4096;
constexpr size_t WS_END = WS_FLAG2 + 8192;

#define STAMPT(ws, kid, i)
constexpr float QSCALE = 0.25f * 1.4426950408889634f;

DI unsigned lds_addr(const void* p) { return (unsigned)(uintptr_t)(LDS_AS const char*)p; }
DI void glds16(const void* sbase, unsigned voff, unsigned ldst) {
  unsigned keep;
  asm volatile("s_mov_b32 %0, m0\n\ts_mov_b32 m0, %3\n\ts_nop 0\n\tglobal_load_lds_dwordx4 %1, %2\n\ts_mov_b32 m0, %0"
               : "=&s"(keep) : "v"(voff), "s"(sbase), "s"(ldst) : "memory");
}
#define WAIT_VM(n) asm volatile("s_waitcnt vmcnt(" #n ")" ::: "memory")
#define WAIT_LGKM0() asm volatile("s_waitcnt lgkmcnt(0)" ::: "memory")
#define BARRIER() __builtin_amdgcn_s_barrier()
#define TBAR(acc) __builtin_amdgcn_s_barrier()
#define TVM(acc, stmt) stmt
#define TBAR_OUT(ws, acc, i)
template <int N> DI void wait_vm() { asm volatile("s_waitcnt vmcnt(%0)" ::"i"(N) : "memory"); }

#define X_STAUX 16
DI __amdgpu_buffer_rsrc_t ws_rsrc(const void* ws) { return __builtin_amdgcn_make_buffer_rsrc((void*)ws, 0, (int)WS_END, 0x00020000); }
template <typename T> DI void st16(__amdgpu_buffer_rsrc_t rs, unsigned off, T v) {
  __builtin_amdgcn_raw_buffer_store_b128(__builtin_bit_cast(u32x4, v), rs, off, 0, X_STAUX);
}
DI bf16x8 lds_b128(const char* sm, unsigned off) { return *(const bf16x8*)(sm + off); }
DI bf16x4 lds_tr(const char* sm, unsigned off) {
  return __builtin_amdgcn_ds_read_tr16_b64_v4bf16((LDS_AS bf16x4*)(LDS_AS char*)(sm + off));
}
DI bf16x8 cat8(bf16x4 lo, bf16x4 hi) { return __builtin_shufflevector(lo, hi, 0, 1, 2, 3, 4, 5, 6, 7); }
DI bf16x8 pack_lo(const f32x16& v) { return __builtin_convertvector(__builtin_shufflevector(v, v, 0, 1, 2, 3, 4, 5, 6, 7), bf16x8); }
DI bf16x8 pack_hi(const f32x16& v) { return __builtin_convertvector(__builtin_shufflevector(v, v, 8, 9, 10, 11, 12, 13, 14, 15), bf16x8); }
DI float bf_lo(unsigned u) { return __uint_as_float(u << 16); }
DI float bf_hi(unsigned u) { return __uint_as_float(u & 0xffff0000u); }
DI int vslot(int d, int h) {
  int s = (d < 4) ? d : (d >= 12) ? d - 8 : (d < 11) ? d + 4 : -1;
  return (s >= 0) ? (16 * h + s) : (32 + h);
}

DI LDS_AS unsigned* lds_word(char* sm, int off) { return (LDS_AS unsigned*)(LDS_AS char*)(sm + off); }
DI void flag_add(char* sm, int off, int lane) {
  if (lane == 0) __hip_atomic_fetch_add(lds_word(sm, off), 1u, __ATOMIC_RELAXED, __HIP_MEMORY_SCOPE_WORKGROUP);
  asm volatile("" ::: "memory");
}
DI unsigned flag_peek(char* sm, int off) { return __hip_atomic_load(lds_word(sm, off), __ATOMIC_RELAXED, __HIP_MEMORY_SCOPE_WORKGROUP); }
DI void flag_wait(char* sm, int off, unsigned target) {
  while ((unsigned)__builtin_amdgcn_readfirstlane(flag_peek(sm, off)) < target) __builtin_amdgcn_s_sleep(1);
  asm volatile("" ::: "memory");
}

constexpr int K1_SLOT = 28672, K1_WOFF = 16384, K1_NSLOT = 5;
constexpr int K1_STATS = K1_SLOT * K1_NSLOT;
constexpr int K1_WPART = K1_STATS + 1024;
constexpr int K1_FLG = K1_WPART + 768;
constexpr int K1_SIDE = K1_FLG + 256;
constexpr int K1_LDS = K1_SIDE + 16384;
#define K1_READY(s) (K1_FLG + 4 * (s))
#define K1_DONE(s) (K1_FLG + 32 + 4 * (s))
#define K1_FIN (K1_FLG + 44)

template <int NV>
DI void k1_gemm_wave(char* sm, int w, int lane, int rg, int cg, char* ws) {
  const int h = lane >> 5, r31 = lane & 31;
  STAMPT(ws, 1, 0);
  unsigned aoff[4];
  {
    const int row = 32 * w + r31;
#pragma unroll
    for (int ks = 0; ks < 4; ++ks) aoff[ks] = row * 128 + (((2 * ks + h) ^ ((row >> 1) & 7)) << 4);
  }
  const unsigned boff = K1_WOFF + (8 * h + ((lane & 15) >> 2)) * 192 + (16 * ((lane >> 4) & 1) + 4 * (lane & 3)) * 2;
  f32x16 acc[3] = {};
  bf16x8 fa[4], fb[4][3];
#pragma unroll
  for (int i = 2; i < 4; ++i) {
    fa[i] = bf16x8{};
#pragma unroll
    for (int j = 0; j < 3; ++j) fb[i][j] = bf16x8{};
  }
#define K1_MMA(ps)                                                        \
  _Pragma("unroll") for (int j = 0; j < 3; ++j) {                         \
    if (j >= 3 - NV) acc[j] = MFMA(fa[ps], fb[ps][j], acc[j]);   \
    else acc[j] = MFMA(fb[ps][j], fa[ps], acc[j]);               \
  }
  int slot = 0;
  unsigned rdy = 0;
#pragma nounroll
  for (int s = 0; s < 8; ++s) {
    if ((unsigned)__builtin_amdgcn_readfirstlane(rdy) < 4u) flag_wait(sm, K1_READY(s), 4);
    asm volatile("" ::: "memory");
    if (s == 0) STAMPT(ws, 1, 1);
    if (s == 4) STAMPT(ws, 1, 3);
    const char* sl = sm + slot;
#pragma unroll
    for (int ks = 0; ks < 4; ++ks) {
      if (ks == 2) rdy = flag_peek(sm, K1_READY(s + 1));
      fa[ks] = lds_b128(sl, aoff[ks]);
#pragma unroll
      for (int j = 0; j < 3; ++j)
        fb[ks][j] = cat8(lds_tr(sl, boff + ks * 16 * 192 + 64 * j), lds_tr(sl, boff + (ks * 16 + 4) * 192 + 64 * j));
      __builtin_amdgcn_sched_barrier(0);
      K1_MMA((ks + 2) & 3);
      __builtin_amdgcn_sched_barrier(0);
      if (ks == 1 && s >= 1 && s <= 3) flag_add(sm, K1_DONE(s - 1), lane);
    }
    slot += K1_SLOT;
    if (slot == K1_NSLOT * K1_SLOT) slot = 0;
  }
  K1_MMA(2);
  K1_MMA(3);
  asm volatile("" : "+v"(acc[0]), "+v"(acc[1]), "+v"(acc[2]));
  STAMPT(ws, 1, 5);
  flag_wait(sm, K1_FIN, 4);
  STAMPT(ws, 1, 6);
  const int rt = rg * 4 + w;
  const __amdgpu_buffer_rsrc_t rs = ws_rsrc(ws);
  const f32x2* stats = (const f32x2*)(sm + K1_STATS) + 32 * w;
  const float* wsum = (const float*)(sm + K1_WPART);
#pragma unroll
  for (int j = 0; j < 3; ++j) {
    const int ct = cg * 3 + j, role = ct >> 4, hp = (ct & 15) * 2;
    f32x16 v;
    if (j >= 3 - NV) {
      const float wsn = wsum[32 * j + r31] + wsum[96 + 32 * j + r31];
#pragma unroll
      for (int g = 0; g < 4; ++g) {
        f32x4 s0 = *(const f32x4*)(stats + 8 * g + 4 * h), s1 = *(const f32x4*)(stats + 8 * g + 4 * h + 2);
        v[4 * g + 0] = acc[j][4 * g + 0] * s0[0] + s0[1] * wsn;
        v[4 * g + 1] = acc[j][4 * g + 1] * s0[2] + s0[3] * wsn;
        v[4 * g + 2] = acc[j][4 * g + 2] * s1[0] + s1[1] * wsn;
        v[4 * g + 3] = acc[j][4 * g + 3] * s1[2] + s1[3] * wsn;
      }
      const unsigned base = (unsigned)WS_KV + (hp + (r31 >> 4)) * KVHEAD + rt * KVTILE + 1024;
      const int sl = vslot(r31 & 15, h);
      st16(rs, base + sl * 16, pack_lo(v));
      st16(rs, base + VBLK + sl * 16, pack_hi(v));
      if ((lane & 15) == 0)
        st16(rs, base + h * VBLK + 15 * 16, u32x4{0x3f803f80u, 0x3f803f80u, 0x3f803f80u, 0x3f803f80u});
    } else {
      f32x2 st = stats[r31];
      if (role == 0) { st[0] *= QSCALE; st[1] *= QSCALE; }
#pragma unroll
      for (int g = 0; g < 4; ++g) {
        f32x4 wsv = *(const f32x4*)(wsum + 32 * j + 8 * g + 4 * h) + *(const f32x4*)(wsum + 96 + 32 * j + 8 * g + 4 * h);
#pragma unroll
        for (int e = 0; e < 4; ++e) v[4 * g + e] = acc[j][4 * g + e] * st[0] + st[1] * wsv[e];
      }
      if (role == 0) {
        const unsigned base = (unsigned)WS_QF + (hp * 64 + rt) * 1024 + lane * 16;
        st16(rs, base, pack_lo(v));
        st16(rs, base + 64 * 1024, pack_hi(v));
      } else {
        const unsigned base = (unsigned)WS_KV + hp * KVHEAD + rt * KVTILE + lane * 16;
        st16(rs, base, pack_lo(v));
        st16(rs, base + KVHEAD, pack_hi(v));
      }
    }
  }
  STAMPT(ws, 1, 7);
}

#define MFMA16(a, b, c) __builtin_amdgcn_mfma_f32_16x16x32_bf16((a), (b), (c), 0, 0, 0)
DI void k1_stats_wave(char* sm, const char* __restrict__ xg, int hw, int lane, int rg, int cg, char* ws) {
  STAMPT(ws, 1, 0);
  const int r15 = lane & 15, kq = lane >> 4;
  unsigned aoff[4];
#pragma unroll
  for (int i = 0; i < 4; ++i) {
    const int row = 32 * hw + 16 * (i >> 1) + r15, c = 4 * (i & 1) + kq;
    aoff[i] = row * 128 + ((c ^ ((row >> 1) & 7)) << 4);
  }
  const unsigned boff = K1_WOFF + (32 * (hw & 1) + 8 * kq + (r15 >> 2)) * 192 + (48 * (hw >> 1) + 4 * (lane & 3)) * 2;
  const bf16x8 ones = __builtin_bit_cast(bf16x8, u32x4{0x3f803f80u, 0x3f803f80u, 0x3f803f80u, 0x3f803f80u});
  f32x4 gram[2] = {}, rsum[2] = {}, wacc[3] = {};
  bf16x8 xa[2][4] = {}, wb[2][3] = {};
  int slot = 0;
  u32x4 xr2[2];
#pragma unroll
  for (int i = 0; i < 2; ++i)
    xr2[i] = *(const u32x4*)(xg + (size_t)(rg * 128 + cg * 8 + (lane & 7)) * 1024 + ((lane >> 3) + 8 * (2 * hw + i)) * 16);
  auto math = [&](const bf16x8 (&xp)[4], const bf16x8 (&wp)[3]) {
#pragma unroll
    for (int i = 0; i < 4; ++i) {
      gram[i >> 1] = MFMA16(xp[i], xp[i], gram[i >> 1]);
      rsum[i >> 1] = MFMA16(xp[i], ones, rsum[i >> 1]);
    }
#pragma unroll
    for (int j = 0; j < 3; ++j) wacc[j] = MFMA16(ones, wp[j], wacc[j]);
  };
#pragma nounroll
  for (int s2 = 0; s2 < 4; ++s2) {
#pragma unroll
    for (int par = 0; par < 2; ++par) {
      flag_wait(sm, K1_READY(2 * s2 + par), 4);
      const char* sl = sm + slot;
#pragma unroll
      for (int i = 0; i < 4; ++i) xa[par][i] = lds_b128(sl, aoff[i]);
#pragma unroll
      for (int j = 0; j < 3; ++j) wb[par][j] = cat8(lds_tr(sl, boff + 32 * j), lds_tr(sl, boff + 4 * 192 + 32 * j));
      __builtin_amdgcn_sched_barrier(0);
      math(xa[par ^ 1], wb[par ^ 1]);
      __builtin_amdgcn_sched_barrier(0);
      WAIT_LGKM0();
      if (2 * s2 + par <= 2) flag_add(sm, K1_DONE(2 * s2 + par), lane);
      slot += K1_SLOT;
      if (slot == K1_NSLOT * K1_SLOT) slot = 0;
    }
  }
  math(xa[1], wb[1]);
  STAMPT(ws, 1, 5);
  if (lane < 16) {
#pragma unroll
    for (int j = 0; j < 3; ++j) *(float*)(sm + K1_WPART + ((hw & 1) * 96 + 48 * (hw >> 1) + 16 * j + lane) * 4) = wacc[j][0];
  }
  if ((r15 >> 2) == kq) {
#pragma unroll
    for (int g2 = 0; g2 < 2; ++g2) {
      const int e = r15 & 3;
      const float sq = e == 0 ? gram[g2][0] : e == 1 ? gram[g2][1] : e == 2 ? gram[g2][2] : gram[g2][3];
      const float sum = e == 0 ? rsum[g2][0] : e == 1 ? rsum[g2][1] : e == 2 ? rsum[g2][2] : rsum[g2][3];
      const int row = 32 * hw + 16 * g2 + r15;
      const float mean = sum * (1.f / 512.f);
      float var = sq * (1.f / 512.f) - mean * mean;
      if (!(var > 1e-3f * mean * mean)) {
        const unsigned* xr = (const unsigned*)(xg + (size_t)(rg * 128 + row) * 1024);
        float acc = 0.f;
        for (int i = 0; i < 256; ++i) {
          const unsigned u = xr[i];
          const float a = bf_lo(u) - mean, b = bf_hi(u) - mean;
          acc = fmaf(a, a, fmaf(b, b, acc));
        }
        var = acc * (1.f / 512.f);
      }
      const float rstd = 1.0f / sqrtf(var + 1e-5f);
      *(f32x2*)(sm + K1_STATS + row * 8) = f32x2{rstd, -mean * rstd};
    }
  }
  WAIT_LGKM0();
  flag_add(sm, K1_FIN, lane);
  flag_wait(sm, K1_FIN, 4);
  STAMPT(ws, 1, 6);
  {
    const int p = lane & 7, lrow = cg * 8 + p;
    const f32x2 st = *(const f32x2*)(sm + K1_STATS + lrow * 8);
    const int grow = rg * 128 + lrow;
#pragma unroll
    for (int i = 0; i < 2; ++i) {
      const int c = (lane >> 3) + 8 * (2 * hw + i);
      const u32x4 u = xr2[i];
      f32x8 v;
#pragma unroll
      for (int e = 0; e < 4; ++e) { v[2 * e] = bf_lo(u[e]) * st[0] + st[1]; v[2 * e + 1] = bf_hi(u[e]) * st[0] + st[1]; }
      st16(ws_rsrc(ws), (unsigned)WS_XN + (((grow >> 5) * 32 + (c >> 1)) * 64 + (c & 1) * 32 + (grow & 31)) * 16,
           __builtin_convertvector(v, bf16x8));
    }
  }
  STAMPT(ws, 1, 7);
}

DI void k1_loader_wave(char* sm, unsigned sbase, const char* xg, const char* wqkv, const char* w1, const char* w2,
                       const char* wo, int lw, int lane, int bid, int rg, int cg, int rot, char* ws) {
  unsigned xoff[4], woff[3];
#pragma unroll
  for (int p = 0; p < 4; ++p) {
    int c = (lane & 7) ^ ((4 * p + (lane >> 4)) & 7);
    xoff[p] = (unsigned)((rg * 128 + 32 * lw + 8 * p + (lane >> 3)) * 1024 + c * 16);
  }
#pragma unroll
  for (int i = 0; i < 3; ++i) {
    int idx = 64 * (3 * lw + i) + lane;
    int row = idx / 12, ch = idx - row * 12;
    woff[i] = (unsigned)(row * 3072 + cg * 192 + ch * 16);
  }
  auto issue = [&](int s, int slot) {
    const int ks = (s + rot) & 7;
    const unsigned dst = sbase + slot;
#pragma unroll
    for (int i = 0; i < 4; ++i) glds16(xg + ks * 128, xoff[i], dst + lw * 4096 + i * 1024);
#pragma unroll
    for (int i = 0; i < 3; ++i) glds16(wqkv + (size_t)ks * 64 * 3072, woff[i], dst + K1_WOFF + lw * 3072 + i * 1024);
  };
  issue(0, 0);
  issue(1, K1_SLOT);
  const char* swsrc; int sld, kb, nb, sks; size_t sdst; bool perm;
  if (bid < 112) { swsrc = w1; sld = FF_ * 2; kb = bid / 14; nb = bid - kb * 14; sks = 32; sdst = WS_W1; perm = false; }
  else if (bid < 224) { int v = bid - 112; swsrc = w2; sld = D_ * 2; kb = v >> 2; nb = v & 3; sks = 112; sdst = WS_W2; perm = true; }
  else { int v = bid - 224; swsrc = wo; sld = D_ * 2; kb = v >> 2; nb = v & 3; sks = 32; sdst = WS_WO; perm = true; }
  const int h = lane >> 5, q = (lane & 15) >> 2;
  const unsigned cb = (16 * ((lane >> 4) & 1) + 4 * (lane & 3)) * 2;
  WAIT_LGKM0();
  BARRIER();
  {
    int islot = 2 * K1_SLOT;
#pragma nounroll
    for (int s = 2; s < 8; ++s) {
      wait_vm<7>(); flag_add(sm, K1_READY(s - 2), lane);
      if (s >= 5) flag_wait(sm, K1_DONE(s - 5), 8);
      issue(s, islot);
      islot += K1_SLOT;
      if (islot == K1_NSLOT * K1_SLOT) islot = 0;
    }
  }
  wait_vm<7>(); flag_add(sm, K1_READY(6), lane);
#pragma unroll
  for (int i = 0; i < 4; ++i) {
    const int p = 4 * lw + i;
    glds16(swsrc + (size_t)(kb * 64 + 4 * p) * sld + nb * 256, (unsigned)((lane >> 4) * sld + (lane & 15) * 16),
           sbase + K1_SIDE + p * 1024);
  }
  wait_vm<4>(); flag_add(sm, K1_READY(7), lane);
  WAIT_VM(0);
  {
    const int r0 = perm ? 4 * h : 8 * h, r1 = perm ? 8 + 4 * h : 8 * h + 4;
    const unsigned o0 = K1_SIDE + (16 * lw + r0 + q) * 256 + cb, o1 = K1_SIDE + (16 * lw + r1 + q) * 256 + cb;
#pragma unroll
    for (int nt = 0; nt < 4; ++nt) {
      bf16x8 f = cat8(lds_tr(sm, o0 + nt * 64), lds_tr(sm, o1 + nt * 64));
      st16(ws_rsrc(ws), (unsigned)sdst + ((nb * 4 + nt) * sks + kb * 4 + lw) * 1024 + lane * 16, f);
    }
  }
}

__global__ __launch_bounds__(768) void k1_qkv(const char* __restrict__ x, const char* __restrict__ wqkv,
                                              const char* __restrict__ w1, const char* __restrict__ w2,
                                              const char* __restrict__ wo, char* __restrict__ ws) {
  extern __shared__ __attribute__((aligned(16))) char sm[];
  const int lane = threadIdx.x & 63, wave = __builtin_amdgcn_readfirstlane(threadIdx.x >> 6);
  const int bid = blockIdx.x, xcd = bid & 7, idx = bid >> 3;
  const int rg = 4 * (xcd >> 1) + (idx & 3), cg = 8 * (xcd & 1) + (idx >> 2);
  if (threadIdx.x >= 256 && threadIdx.x < 272) *(unsigned*)(sm + K1_FLG + (threadIdx.x - 256) * 4) = 0u;
  if (wave >= 4) { WAIT_LGKM0(); BARRIER(); }
  if (wave < 4) {
    const int rot = ((idx >> 2) + 2 * (idx & 3)) & 7;
    k1_loader_wave(sm, lds_addr(sm), x, wqkv, w1, w2, wo, wave, lane, bid, rg, cg, rot, ws);
  } else if (wave < 8) {
    if (cg < 10) k1_gemm_wave<0>(sm, wave - 4, lane, rg, cg, ws);
    else if (cg == 10) k1_gemm_wave<1>(sm, wave - 4, lane, rg, cg, ws);
    else k1_gemm_wave<3>(sm, wave - 4, lane, rg, cg, ws);
  } else {
    if (bid == 0) st16(ws_rsrc(ws), (unsigned)WS_FLAG + (wave - 8) * 1024 + lane * 16, u32x4{0u, 0u, 0u, 0u});
#pragma unroll
    for (int i = 0; i < 2; ++i)
      st16(ws_rsrc(ws), (unsigned)WS_OF + bid * 8192 + (wave - 8) * 2048 + i * 1024 + lane * 16, u32x4{~0u, ~0u, ~0u, ~0u});
    k1_stats_wave(sm, x, wave - 8, lane, rg, cg, ws);
  }
}

constexpr int K2_FLG = KVHEAD;
constexpr int K2_LDS = K2_FLG + 64;
constexpr float SM_DELTA = 60.f;
typedef __attribute__((address_space(1))) unsigned gu32;
#define X_AUXB 16
#define SB() __builtin_amdgcn_sched_barrier(0)

DI float xhalf_max(float v) {
  auto r = __builtin_amdgcn_permlane32_swap(__float_as_uint(v), __float_as_uint(v), false, false);
  return fmaxf(__uint_as_float(r[0]), __uint_as_float(r[1]));
}
DI float max16(const f32x16& s) {
  float m01 = fmaxf(fmaxf(s[0], s[1]), s[2]), m23 = fmaxf(fmaxf(s[3], s[4]), s[5]);
  float m45 = fmaxf(fmaxf(s[6], s[7]), s[8]), m67 = fmaxf(fmaxf(s[9], s[10]), s[11]);
  float m89 = fmaxf(fmaxf(s[12], s[13]), s[14]);
  return fmaxf(fmaxf(fmaxf(m01, m23), m45), fmaxf(fmaxf(m67, m89), s[15]));
}
DI void lds_signal(char* sm, int word, int lane) { flag_add(sm, K2_FLG + 4 * word, lane); }
DI void lds_wait(char* sm, int word, unsigned target) { flag_wait(sm, K2_FLG + 4 * word, target); }

template <int NKS, int PF, int AUXB>
DI void dgemm64_ring(__amdgpu_buffer_rsrc_t rs, unsigned a0, unsigned a1, unsigned b0, unsigned b1, int rot, f32x16 (&acc)[4]) {
  u32x4 A0[PF], A1[PF], B0[PF], B1[PF];
  int ko = rot * 1024;
#pragma unroll
  for (int kk = 0; kk < NKS + PF - 1; ++kk) {
    if (kk < NKS) {
      const int sl = kk % PF;
      A0[sl] = __builtin_amdgcn_raw_buffer_load_b128(rs, a0, ko, 0);
      B0[sl] = __builtin_amdgcn_raw_buffer_load_b128(rs, b0, ko, AUXB);
      A1[sl] = __builtin_amdgcn_raw_buffer_load_b128(rs, a1, ko, 0);
      B1[sl] = __builtin_amdgcn_raw_buffer_load_b128(rs, b1, ko, AUXB);
      ko += 1024;
      if (ko == NKS * 1024) ko = 0;
    }
    SB();
    if (kk >= PF - 1) {
      const int sl = (kk - (PF - 1)) % PF;
      const bf16x8 fa0 = __builtin_bit_cast(bf16x8, A0[sl]), fa1 = __builtin_bit_cast(bf16x8, A1[sl]);
      const bf16x8 fb0 = __builtin_bit_cast(bf16x8, B0[sl]), fb1 = __builtin_bit_cast(bf16x8, B1[sl]);
#define X_GAP
      acc[0] = MFMA(fa0, fb0, acc[0]);
      SB(); X_GAP; SB();
      acc[2] = MFMA(fa1, fb0, acc[2]);
      SB(); X_GAP; SB();
      acc[1] = MFMA(fa0, fb1, acc[1]);
      SB(); X_GAP; SB();
      acc[3] = MFMA(fa1, fb1, acc[3]);
    }
    SB();
  }
  asm volatile("" : "+v"(acc[0]), "+v"(acc[1]), "+v"(acc[2]), "+v"(acc[3]));
}

DI void k2_ffn_wave(char* sm, const char* __restrict__ x, char* __restrict__ ws, char* __restrict__ out, int w, int lane, int panel, int ci, int head) {
  {
    const unsigned sb = lds_addr(sm) + w * 1024, dl = lane * 16;
    const char* kvg = ws + WS_KV + (size_t)head * KVHEAD + w * 1024;
#pragma unroll
    for (int i = 0; i < 8; ++i) glds16(kvg + i * 4096, dl, sb + i * 4096);
    WAIT_LGKM0();
    BARRIER();
    WAIT_VM(4);
    lds_signal(sm, 0, lane);
#pragma unroll
    for (int i = 8; i < 34; ++i) glds16(kvg + i * 4096, dl, sb + i * 4096);
    WAIT_VM(22);
    lds_signal(sm, 1, lane);
    WAIT_VM(0);
    lds_signal(sm, 2, lane);
  }
#define X_FPRIO 2
  __builtin_amdgcn_s_setprio(X_FPRIO);
  const __amdgpu_buffer_rsrc_t rs = __builtin_amdgcn_make_buffer_rsrc((void*)ws, 0, (int)WS_END, 0x00020000);
  gu32* flags = (gu32*)(ws + WS_FLAG) + panel * 128;
  STAMPT(ws, 2, 0);
  if (ci < 28) {
    const int rt0 = panel * 8 + 2 * w, nt0 = 2 * ci;
    const unsigned a0 = (unsigned)WS_W1 + nt0 * 32768 + lane * 16, b0 = (unsigned)WS_XN + rt0 * 32768 + lane * 16;
    f32x16 acc[4] = {};
#define X_ROT1 ci
    dgemm64_ring<32, 5, 0>(rs, a0, a0 + 32768, b0, b0 + 32768, X_ROT1, acc);
    STAMPT(ws, 2, 5);
#pragma unroll
    for (int i = 0; i < 2; ++i)
#pragma unroll
      for (int j = 0; j < 2; ++j) {
        f32x16 v = acc[2 * i + j];
#pragma unroll
        for (int e = 0; e < 16; ++e) v[e] = fmaxf(v[e], 0.f);
        const unsigned dst = (unsigned)WS_HF + ((rt0 + j) * 112 + (nt0 + i) * 2) * 1024 + lane * 16;
        __builtin_amdgcn_raw_buffer_store_b128(__builtin_bit_cast(u32x4, pack_lo(v)), rs, dst, 0, 16);
        __builtin_amdgcn_raw_buffer_store_b128(__builtin_bit_cast(u32x4, pack_hi(v)), rs, dst + 1024, 0, 16);
      }
    WAIT_VM(0);
    if (lane == 0) __hip_atomic_store(flags + w * 32 + ci, 1u, __ATOMIC_RELAXED, __HIP_MEMORY_SCOPE_AGENT);
  }
  STAMPT(ws, 2, 1);
  const int rp2 = ci & 3, np2 = ci >> 2;
  {
    gu32* f = flags + rp2 * 32 + 7 * w + min(lane, 6);
    for (unsigned spins = 0; spins < (1u << 20); ++spins) {
      unsigned v = __hip_atomic_load(f, __ATOMIC_RELAXED, __HIP_MEMORY_SCOPE_AGENT);
      if (__all(v == 1u)) break;
      __builtin_amdgcn_s_sleep(4);
    }
    __builtin_amdgcn_fence(__ATOMIC_ACQUIRE, "wavefront");
    asm volatile("" ::: "memory");
  }
  STAMPT(ws, 2, 2);
  const int rt0 = panel * 8 + 2 * rp2, nt0 = 2 * np2;
  f32x16 acc[4] = {};
  {
    const unsigned a0 = (unsigned)WS_W2 + (nt0 * 112 + 28 * w) * 1024 + lane * 16;
    const unsigned b0 = (unsigned)WS_HF + (rt0 * 112 + 28 * w) * 1024 + lane * 16;
    dgemm64_ring<28, 5, X_AUXB>(rs, a0, a0 + 112 * 1024, b0, b0 + 112 * 1024, (7 * rp2 + 3 * np2) % 28, acc);
  }
  STAMPT(ws, 2, 3);
  const int h = lane >> 5, r31 = lane & 31;
  const size_t eoff = ((size_t)((rt0 + (w & 1)) * 32 + r31) * D_ + (nt0 + (w >> 1)) * 32 + 4 * h) * 2;
  u32x2 xv[4];
#pragma unroll
  for (int g = 0; g < 4; ++g) xv[g] = *(const u32x2*)(x + eoff + 16 * g);
  lds_wait(sm, 3, 8);
  {
    const int wl = 65536 + w * 16384;
    const char* wog = ws + WS_WO + (size_t)(nt0 * 32 + 8 * w) * 1024;
#pragma unroll
    for (int i = 0; i < 16; ++i) glds16(wog + (i >> 3) * 32768 + (i & 7) * 1024, lane * 16, lds_addr(sm) + wl + i * 1024);
    gu32* f = (gu32*)(ws + WS_OF + ((rt0 + ((lane >> 3) & 1)) * 32 + 8 * w + (lane & 7)) * 1024 + 1020);
    for (unsigned spins = 0; spins < (1u << 20); ++spins) {
      unsigned v = __hip_atomic_load(f, __ATOMIC_RELAXED, __HIP_MEMORY_SCOPE_AGENT);
      if (__all(v != ~0u)) break;
      __builtin_amdgcn_s_sleep(2);
    }
    asm volatile("" ::: "memory");
    STAMPT(ws, 2, 6);
    const unsigned b0 = (unsigned)WS_OF + (rt0 * 32 + 8 * w) * 1024 + lane * 16;
    u32x4 B[16];
    for (unsigned spins = 0; spins < (1u << 16); ++spins) {
      asm volatile("" ::: "memory");
#pragma unroll
      for (int i = 0; i < 16; ++i) B[i] = __builtin_amdgcn_raw_buffer_load_b128(rs, b0, (i & 1) * 32768 + (i >> 1) * 1024, 16);
      unsigned m = 0;
#pragma unroll
      for (int i = 0; i < 16; ++i) m = max(max(m, B[i][0]), max(B[i][1], max(B[i][2], B[i][3])));
      if (__builtin_expect(!__any(m == ~0u), 1)) break;
    }
    WAIT_VM(0);
#pragma unroll
    for (int ks = 0; ks < 8; ++ks) {
      const bf16x8 fa0 = lds_b128(sm, wl + ks * 1024 + lane * 16), fa1 = lds_b128(sm, wl + 8192 + ks * 1024 + lane * 16);
      const bf16x8 fb0 = __builtin_bit_cast(bf16x8, B[2 * ks]), fb1 = __builtin_bit_cast(bf16x8, B[2 * ks + 1]);
      acc[0] = MFMA(fa0, fb0, acc[0]);
      acc[2] = MFMA(fa1, fb0, acc[2]);
      acc[1] = MFMA(fa0, fb1, acc[1]);
      acc[3] = MFMA(fa1, fb1, acc[3]);
    }
  }
  {
    f32x4* red = (f32x4*)sm + lane;
#pragma unroll
    for (int t = 0; t < 4; ++t)
#pragma unroll
      for (int g = 0; g < 4; ++g)
        red[((t * 4 + w) * 4 + g) * 64] = f32x4{acc[t][4 * g], acc[t][4 * g + 1], acc[t][4 * g + 2], acc[t][4 * g + 3]};
    WAIT_LGKM0();
    lds_signal(sm, 4, lane);
    lds_wait(sm, 4, 4);
#pragma unroll
    for (int g = 0; g < 4; ++g) {
      f32x4 v = red[((w * 4 + 0) * 4 + g) * 64];
#pragma unroll
      for (int src = 1; src < 4; ++src) v += red[((w * 4 + src) * 4 + g) * 64];
      v[0] += bf_lo(xv[g][0]); v[1] += bf_hi(xv[g][0]); v[2] += bf_lo(xv[g][1]); v[3] += bf_hi(xv[g][1]);
      *(bf16x4*)(out + eoff + 16 * g) = __builtin_convertvector(v, bf16x4);
    }
  }
  STAMPT(ws, 2, 4);
}

__global__ __launch_bounds__(768) void k2_attn(const char* __restrict__ x, char* __restrict__ ws, char* __restrict__ out) {
  extern __shared__ __attribute__((aligned(16))) char sm[];
  const int lane = threadIdx.x & 63, wave = __builtin_amdgcn_readfirstlane(threadIdx.x >> 6);
  const int bid = blockIdx.x, xcd = bid & 7, idx = bid >> 3;
  if (threadIdx.x < 16) *(unsigned*)(sm + K2_FLG + threadIdx.x * 4) = 0u;
  const int head = xcd * 4 + (idx >> 3), qb = idx & 7, rt = qb * 8 + wave;
  if (wave >= 8) {
    k2_ffn_wave(sm, x, ws, out, wave - 8, lane, xcd, idx, head);
    return;
  }
  STAMPT(ws, 2, 0);
  const bf16x8 qf = *((const bf16x8*)(ws + WS_QF) + ((size_t)(head * 64 + rt) * 64 + lane));
  const int h = lane >> 5, r31 = lane & 31;
  const unsigned koff = lane * 16;
  const unsigned voff = 1024 + ((r31 < 16) ? vslot(r31, h) : 15) * 16;
  WAIT_LGKM0();
  BARRIER();
  f32x16 negm = {}, O = {};
#define EXP(v, i) v[i] = __builtin_amdgcn_exp2f(v[i])
#define TILE_STEP(SC, SN, OFFK2, OFFV0N, OFFV1)                                   \
  {                                                                               \
    SN = MFMA(kn, qf, negm);                                                      \
    SB();                                                                         \
    kn = lds_b128(kp, OFFK2);                                                     \
    EXP(SC, 0); EXP(SC, 1); EXP(SC, 2); EXP(SC, 3); EXP(SC, 4); EXP(SC, 5);       \
    SB();                                                                         \
    O = MFMA(v1p, p1p, O);                                                        \
    SB();                                                                         \
    v1p = lds_b128(vp, OFFV1);                                                    \
    EXP(SC, 6); EXP(SC, 7);                                                       \
    bf16x8 p0 = pack_lo(SC);                                                      \
    SB();                                                                         \
    EXP(SC, 8); EXP(SC, 9);                                                       \
    SB();                                                                         \
    O = MFMA(v0, p0, O);                                                          \
    SB();                                                                         \
    v0 = lds_b128(vp, OFFV0N);                                                    \
    EXP(SC, 10); EXP(SC, 11); EXP(SC, 12); EXP(SC, 13); EXP(SC, 14); EXP(SC, 15); \
    p1p = pack_hi(SC);                                                            \
    SB();                                                                         \
  }
#define TILE4()                                                        \
  TILE_STEP(sA, sB, 2 * KVTILE, 1 * KVTILE, 0 * KVTILE + VBLK);        \
  TILE_STEP(sB, sA, 3 * KVTILE, 2 * KVTILE, 1 * KVTILE + VBLK);        \
  TILE_STEP(sA, sB, 4 * KVTILE, 3 * KVTILE, 2 * KVTILE + VBLK);        \
  TILE_STEP(sB, sA, 5 * KVTILE, 4 * KVTILE, 3 * KVTILE + VBLK);        \
  kp += 4 * KVTILE; vp += 4 * KVTILE;
  {
    const char* kp = sm + koff;
    const char* vp = sm + voff;
    f32x16 sA = {}, sB;
    bf16x8 kn = {}, v0 = {}, v1p = {}, p1p = {};
#define X_SW 16
    int ev = 0;
    for (int t = 0; t < 64; t += 4) {
      if (__builtin_expect(t == ev, 0)) {
        if (t <= 0) {
          if (t == 0) { lds_wait(sm, 0, 4); STAMPT(ws, 2, 1); }
          kp = sm + koff; vp = sm + voff;
          const f32x16 z = {};
          const bf16x8 k0 = lds_b128(kp, 0);
          kn = lds_b128(kp, KVTILE);
          v0 = lds_b128(vp, 0);
          v1p = lds_b128(vp, VBLK);
          f32x16 s0 = MFMA(k0, qf, z), s1 = MFMA(kn, qf, z);
          const float nm = -(xhalf_max(fmaxf(max16(s0), max16(s1))) + SM_DELTA);
#pragma unroll
          for (int e = 0; e < 16; ++e) { negm[e] = nm; O[e] = 0.f; }
          p1p = bf16x8{};
          sA = MFMA(k0, qf, negm);
          ev = t + 4;
        } else {
          lds_wait(sm, t == 4 ? 1 : 2, 4);
          if (t != 4 && wave >= 4) __builtin_amdgcn_s_setprio(1);
          ev = (t == 4) ? X_SW : -100;
        }
      }
      TILE4();
      asm volatile("" : "+v"(O), "+v"(sA), "+v"(p1p));
    }
    O = MFMA(v1p, p1p, O);
  }
  STAMPT(ws, 2, 2);
  float l = O[8];
  bool ok = (l < 0x1p100f);
#pragma unroll
  for (int e = 0; e < 8; ++e) ok = ok && (fabsf(O[e]) < 0x1p126f);
  if (!__all(ok)) {
    float m = -INFINITY;
#pragma unroll
    for (int e = 0; e < 16; ++e) O[e] = 0.f;
    for (int t = 0; t < 64; ++t) {
      f32x16 z = {};
      f32x16 sc = MFMA(lds_b128(sm, koff + t * KVTILE), qf, z);
      bf16x8 v0 = lds_b128(sm, voff + t * KVTILE), v1 = lds_b128(sm, voff + t * KVTILE + VBLK);
      float mn = fmaxf(m, xhalf_max(max16(sc)));
      float alpha = __builtin_amdgcn_exp2f(m - mn);
      m = mn;
#pragma unroll
      for (int e = 0; e < 16; ++e) { O[e] *= alpha; sc[e] = __builtin_amdgcn_exp2f(sc[e] - mn); }
      O = MFMA(v0, pack_lo(sc), O);
      O = MFMA(v1, pack_hi(sc), O);
    }
    l = O[8];
  }
  const float rl = 1.0f / l;
#pragma unroll
  for (int e = 0; e < 8; ++e) O[e] *= rl;
  u32x4 po = __builtin_bit_cast(u32x4, pack_lo(O));
#pragma unroll
  for (int e = 0; e < 4; ++e) po[e] = min(po[e], 0xFFFFFFFEu);
  __builtin_amdgcn_raw_buffer_store_b128(po, ws_rsrc(ws), (unsigned)WS_OF + (rt * 32 + head) * 1024 + lane * 16, 0, 16);
  WAIT_LGKM0();
  lds_signal(sm, 3, lane);
  STAMPT(ws, 2, 3);
}

extern "C" void kernel_launch(void* const* d_in, const int* in_sizes, int n_in, void* d_out, int out_size, void* d_ws,
                              size_t ws_size, hipStream_t stream) {
  const char* x = (const char*)d_in[0];
  const char* wqkv = (const char*)d_in[1];
  const char* wo = (const char*)d_in[2];
  const char* w1 = (const char*)d_in[4];
  const char* w2 = (const char*)d_in[5];
  char* ws = (char*)d_ws;
  (void)hipFuncSetAttribute((const void*)k1_qkv, hipFuncAttributeMaxDynamicSharedMemorySize, K1_LDS);
  (void)hipFuncSetAttribute((const void*)k2_attn, hipFuncAttributeMaxDynamicSharedMemorySize, K2_LDS);
  hipLaunchKernelGGL(k1_qkv, dim3(256), dim3(768), K1_LDS, stream, x, wqkv, w1, w2, wo, ws);
  hipLaunchKernelGGL(k2_attn, dim3(256), dim3(768), K2_LDS, stream, x, ws, (char*)d_out);
}
```

```cpp
#include <hip/hip_runtime.h>
#include <hip/hip_bf16.h>
#include <stdint.h>

typedef __attribute__((ext_vector_type(8))) __bf16 bf16x8;
typedef __attribute__((ext_vector_type(4))) __bf16 bf16x4;
typedef __attribute__((ext_vector_type(16))) float f32x16;
typedef __attribute__((ext_vector_type(8))) float f32x8;
typedef __attribute__((ext_vector_type(4))) float f32x4;
typedef __attribute__((ext_vector_type(2))) float f32x2;
typedef __attribute__((ext_vector_type(4))) unsigned u32x4;
typedef __attribute__((ext_vector_type(2))) unsigned u32x2;
#define LDS_AS __attribute__((address_space(3)))
#define DI __device__ __forceinline__
#define MFMA(a, b, c) __builtin_amdgcn_mfma_f32_32x32x16_bf16((a), (b), (c), 0, 0, 0)

constexpr int S_ = 2048, D_ = 512, FF_ = 1792, NH_ = 32;
constexpr int KVTILE = 2112;
constexpr int VBLK = 544;
constexpr int KVHEAD = 136 * 1024;
constexpr size_t WS_QF = 0;
constexpr size_t WS_KV = WS_QF + (size_t)2 * 1024 * 1024;
constexpr size_t WS_XN = WS_KV + (size_t)NH_ * KVHEAD;
constexpr size_t WS_W1 = WS_XN + (size_t)2 * 1024 * 1024;
constexpr size_t WS_W2 = WS_W1 + (size_t)1792 * 1024;
constexpr size_t WS_WO = WS_W2 + (size_t)1792 * 1024;
constexpr size_t WS_HF = WS_WO + (size_t)512 * 1024;
constexpr size_t WS_OF = WS_HF + (size_t)64 * 112 * 1024;
constexpr size_t WS_Y = WS_OF + (size_t)2 * 1024 * 1024;
constexpr size_t WS_FLAG = WS_Y + (size_t)4 * 1024 * 1024;
constexpr size_t WS_FLAG2 = WS_FLAG + 4096;
constexpr size_t WS_END = WS_FLAG2 + 8192;

#define STAMPT(ws, kid, i)
constexpr float QSCALE = 0.25f * 1.4426950408889634f;

DI unsigned lds_addr(const void* p) { return (unsigned)(uintptr_t)(LDS_AS const char*)p; }
DI void glds16(const void* sbase, unsigned voff, unsigned ldst) {
  unsigned keep;
  asm volatile("s_mov_b32 %0, m0\n\ts_mov_b32 m0, %3\n\ts_nop 0\n\tglobal_load_lds_dwordx4 %1, %2\n\ts_mov_b32 m0, %0"
               : "=&s"(keep) : "v"(voff), "s"(sbase), "s"(ldst) : "memory");
}
#define WAIT_VM(n) asm volatile("s_waitcnt vmcnt(" #n ")" ::: "memory")
#define WAIT_LGKM0() asm volatile("s_waitcnt lgkmcnt(0)" ::: "memory")
#define BARRIER() __builtin_amdgcn_s_barrier()
#define TBAR(acc) __builtin_amdgcn_s_barrier()
#define TVM(acc, stmt) stmt
#define TBAR_OUT(ws, acc, i)
template <int N> DI void wait_vm() { asm volatile("s_waitcnt vmcnt(%0)" ::"i"(N) : "memory"); }

#define X_STAUX 16
DI __amdgpu_buffer_rsrc_t ws_rsrc(const void* ws) { return __builtin_amdgcn_make_buffer_rsrc((void*)ws, 0, (int)WS_END, 0x00020000); }
template <typename T> DI void st16(__amdgpu_buffer_rsrc_t rs, unsigned off, T v) {
  __builtin_amdgcn_raw_buffer_store_b128(__builtin_bit_cast(u32x4, v), rs, off, 0, X_STAUX);
}
DI bf16x8 lds_b128(const char* sm, unsigned off) { return *(const bf16x8*)(sm + off); }
DI bf16x4 lds_tr(const char* sm, unsigned off) {
  return __builtin_amdgcn_ds_read_tr16_b64_v4bf16((LDS_AS bf16x4*)(LDS_AS char*)(sm + off));
}
DI bf16x8 cat8(bf16x4 lo, bf16x4 hi) { return __builtin_shufflevector(lo, hi, 0, 1, 2, 3, 4, 5, 6, 7); }
DI bf16x8 pack_lo(const f32x16& v) { return __builtin_convertvector(__builtin_shufflevector(v, v, 0, 1, 2, 3, 4, 5, 6, 7), bf16x8); }
DI bf16x8 pack_hi(const f32x16& v) { return __builtin_convertvector(__builtin_shufflevector(v, v, 8, 9, 10, 11, 12, 13, 14, 15), bf16x8); }
DI float bf_lo(unsigned u) { return __uint_as_float(u << 16); }
DI float bf_hi(unsigned u) { return __uint_as_float(u & 0xffff0000u); }
DI int vslot(int d, int h) {
  int s = (d < 4) ? d : (d >= 12) ? d - 8 : (d < 11) ? d + 4 : -1;
  return (s >= 0) ? (16 * h + s) : (32 + h);
}

DI LDS_AS unsigned* lds_word(char* sm, int off) { return (LDS_AS unsigned*)(LDS_AS char*)(sm + off); }
DI void flag_add(char* sm, int off, int lane) {
  if (lane == 0) __hip_atomic_fetch_add(lds_word(sm, off), 1u, __ATOMIC_RELAXED, __HIP_MEMORY_SCOPE_WORKGROUP);
  asm volatile("" ::: "memory");
}
DI unsigned flag_peek(char* sm, int off) { return __hip_atomic_load(lds_word(sm, off), __ATOMIC_RELAXED, __HIP_MEMORY_SCOPE_WORKGROUP); }
template <int SLEEP = 1>
DI void flag_wait(char* sm, int off, unsigned target) {
  while ((unsigned)__builtin_amdgcn_readfirstlane(flag_peek(sm, off)) < target) __builtin_amdgcn_s_sleep(SLEEP);
  asm volatile("" ::: "memory");
}

constexpr int K1_SLOT = 28672, K1_WOFF = 16384, K1_NSLOT = 5;
constexpr int K1_STATS = K1_SLOT * K1_NSLOT;
constexpr int K1_WPART = K1_STATS + 1024;
constexpr int K1_FLG = K1_WPART + 768;
constexpr int K1_SIDE = K1_FLG + 256;
constexpr int K1_LDS = K1_SIDE + 16384;
#define K1_READY(s) (K1_FLG + 4 * (s))
#define K1_DONE(s) (K1_FLG + 32 + 4 * (s))
#define K1_FIN (K1_FLG + 44)

template <int NV>
DI void k1_gemm_wave(char* sm, int w, int lane, int rg, int cg, char* ws) {
  const int h = lane >> 5, r31 = lane & 31;
  STAMPT(ws, 1, 0);
  unsigned aoff[4];
  {
    const int row = 32 * w + r31;
#pragma unroll
    for (int ks = 0; ks < 4; ++ks) aoff[ks] = row * 128 + (((2 * ks + h) ^ ((row >> 1) & 7)) << 4);
  }
  const unsigned boff = K1_WOFF + (8 * h + ((lane & 15) >> 2)) * 192 + (16 * ((lane >> 4) & 1) + 4 * (lane & 3)) * 2;
  f32x16 acc[3] = {};
  bf16x8 fa[4], fb[4][3];
#pragma unroll
  for (int i = 2; i < 4; ++i) {
    fa[i] = bf16x8{};
#pragma unroll
    for (int j = 0; j < 3; ++j) fb[i][j] = bf16x8{};
  }
#define K1_MMA(ps)                                                        \
  _Pragma("unroll") for (int j = 0; j < 3; ++j) {                         \
    if (j >= 3 - NV) acc[j] = MFMA(fa[ps], fb[ps][j], acc[j]);   \
    else acc[j] = MFMA(fb[ps][j], fa[ps], acc[j]);               \
  }
  int slot = 0;
  unsigned rdy = 0;
#pragma nounroll
  for (int s = 0; s < 8; ++s) {
    if ((unsigned)__builtin_amdgcn_readfirstlane(rdy) < 4u) flag_wait(sm, K1_READY(s), 4);
    asm volatile("" ::: "memory");
    if (s == 0) STAMPT(ws, 1, 1);
    if (s == 4) STAMPT(ws, 1, 3);
    const char* sl = sm + slot;
#pragma unroll
    for (int ks = 0; ks < 4; ++ks) {
      if (ks == 2) rdy = flag_peek(sm, K1_READY(s + 1));
      fa[ks] = lds_b128(sl, aoff[ks]);
#pragma unroll
      for (int j = 0; j < 3; ++j)
        fb[ks][j] = cat8(lds_tr(sl, boff + ks * 16 * 192 + 64 * j), lds_tr(sl, boff + (ks * 16 + 4) * 192 + 64 * j));
      __builtin_amdgcn_sched_barrier(0);
      K1_MMA((ks + 2) & 3);
      __builtin_amdgcn_sched_barrier(0);
      if (ks == 1 && s >= 1 && s <= 3) flag_add(sm, K1_DONE(s - 1), lane);
    }
    slot += K1_SLOT;
    if (slot == K1_NSLOT * K1_SLOT) slot = 0;
  }
  K1_MMA(2);
  K1_MMA(3);
  asm volatile("" : "+v"(acc[0]), "+v"(acc[1]), "+v"(acc[2]));
  STAMPT(ws, 1, 5);
  flag_wait(sm, K1_FIN, 4);
  STAMPT(ws, 1, 6);
  const int rt = rg * 4 + w;
  const __amdgpu_buffer_rsrc_t rs = ws_rsrc(ws);
  const f32x2* stats = (const f32x2*)(sm + K1_STATS) + 32 * w;
  const float* wsum = (const float*)(sm + K1_WPART);
#pragma unroll
  for (int j = 0; j < 3; ++j) {
    const int ct = cg * 3 + j, role = ct >> 4, hp = (ct & 15) * 2;
    f32x16 v;
    if (j >= 3 - NV) {
      const float wsn = wsum[32 * j + r31] + wsum[96 + 32 * j + r31];
#pragma unroll
      for (int g = 0; g < 4; ++g) {
        f32x4 s0 = *(const f32x4*)(stats + 8 * g + 4 * h), s1 = *(const f32x4*)(stats + 8 * g + 4 * h + 2);
        v[4 * g + 0] = acc[j][4 * g + 0] * s0[0] + s0[1] * wsn;
        v[4 * g + 1] = acc[j][4 * g + 1] * s0[2] + s0[3] * wsn;
        v[4 * g + 2] = acc[j][4 * g + 2] * s1[0] + s1[1] * wsn;
        v[4 * g + 3] = acc[j][4 * g + 3] * s1[2] + s1[3] * wsn;
      }
      const unsigned base = (unsigned)WS_KV + (hp + (r31 >> 4)) * KVHEAD + rt * KVTILE + 1024;
      const int sl = vslot(r31 & 15, h);
      st16(rs, base + sl * 16, pack_lo(v));
      st16(rs, base + VBLK + sl * 16, pack_hi(v));
      if ((lane & 15) == 0)
        st16(rs, base + h * VBLK + 15 * 16, u32x4{0x3f803f80u, 0x3f803f80u, 0x3f803f80u, 0x3f803f80u});
    } else {
      f32x2 st = stats[r31];
      if (role == 0) { st[0] *= QSCALE; st[1] *= QSCALE; }
#pragma unroll
      for (int g = 0; g < 4; ++g) {
        f32x4 wsv = *(const f32x4*)(wsum + 32 * j + 8 * g + 4 * h) + *(const f32x4*)(wsum + 96 + 32 * j + 8 * g + 4 * h);
#pragma unroll
        for (int e = 0; e < 4; ++e) v[4 * g + e] = acc[j][4 * g + e] * st[0] + st[1] * wsv[e];
      }
      if (role == 0) {
        const unsigned base = (unsigned)WS_QF + (hp * 64 + rt) * 1024 + lane * 16;
        st16(rs, base, pack_lo(v));
        st16(rs, base + 64 * 1024, pack_hi(v));
      } else {
        const unsigned base = (unsigned)WS_KV + hp * KVHEAD + rt * KVTILE + lane * 16;
        st16(rs, base, pack_lo(v));
        st16(rs, base + KVHEAD, pack_hi(v));
      }
    }
  }
  STAMPT(ws, 1, 7);
}

#define MFMA16(a, b, c) __builtin_amdgcn_mfma_f32_16x16x32_bf16((a), (b), (c), 0, 0, 0)
DI void k1_stats_wave(char* sm, const char* __restrict__ xg, int hw, int lane, int rg, int cg, char* ws) {
  STAMPT(ws, 1, 0);
  const int r15 = lane & 15, kq = lane >> 4;
  unsigned aoff[4];
#pragma unroll
  for (int i = 0; i < 4; ++i) {
    const int row = 32 * hw + 16 * (i >> 1) + r15, c = 4 * (i & 1) + kq;
    aoff[i] = row * 128 + ((c ^ ((row >> 1) & 7)) << 4);
  }
  const unsigned boff = K1_WOFF + (32 * (hw & 1) + 8 * kq + (r15 >> 2)) * 192 + (48 * (hw >> 1) + 4 * (lane & 3)) * 2;
  const bf16x8 ones = __builtin_bit_cast(bf16x8, u32x4{0x3f803f80u, 0x3f803f80u, 0x3f803f80u, 0x3f803f80u});
  f32x4 gram[2] = {}, rsum[2] = {}, wacc[3] = {};
  bf16x8 xa[2][4] = {}, wb[2][3] = {};
  int slot = 0;
  u32x4 xr2[2];
#pragma unroll
  for (int i = 0; i < 2; ++i)
    xr2[i] = *(const u32x4*)(xg + (size_t)(rg * 128 + cg * 8 + (lane & 7)) * 1024 + ((lane >> 3) + 8 * (2 * hw + i)) * 16);
  auto math = [&](const bf16x8 (&xp)[4], const bf16x8 (&wp)[3]) {
#pragma unroll
    for (int i = 0; i < 4; ++i) {
      gram[i >> 1] = MFMA16(xp[i], xp[i], gram[i >> 1]);
      rsum[i >> 1] = MFMA16(xp[i], ones, rsum[i >> 1]);
    }
#pragma unroll
    for (int j = 0; j < 3; ++j) wacc[j] = MFMA16(ones, wp[j], wacc[j]);
  };
#pragma nounroll
  for (int s2 = 0; s2 < 4; ++s2) {
#pragma unroll
    for (int par = 0; par < 2; ++par) {
      flag_wait(sm, K1_READY(2 * s2 + par), 4);
      const char* sl = sm + slot;
#pragma unroll
      for (int i = 0; i < 4; ++i) xa[par][i] = lds_b128(sl, aoff[i]);
#pragma unroll
      for (int j = 0; j < 3; ++j) wb[par][j] = cat8(lds_tr(sl, boff + 32 * j), lds_tr(sl, boff + 4 * 192 + 32 * j));
      __builtin_amdgcn_sched_barrier(0);
      math(xa[par ^ 1], wb[par ^ 1]);
      __builtin_amdgcn_sched_barrier(0);
      WAIT_LGKM0();
      if (2 * s2 + par <= 2) flag_add(sm, K1_DONE(2 * s2 + par), lane);
      slot += K1_SLOT;
      if (slot == K1_NSLOT * K1_SLOT) slot = 0;
    }
  }
  math(xa[1], wb[1]);
  STAMPT(ws, 1, 5);
  if (lane < 16) {
#pragma unroll
    for (int j = 0; j < 3; ++j) *(float*)(sm + K1_WPART + ((hw & 1) * 96 + 48 * (hw >> 1) + 16 * j + lane) * 4) = wacc[j][0];
  }
  if ((r15 >> 2) == kq) {
#pragma unroll
    for (int g2 = 0; g2 < 2; ++g2) {
      const int e = r15 & 3;
      const float sq = e == 0 ? gram[g2][0] : e == 1 ? gram[g2][1] : e == 2 ? gram[g2][2] : gram[g2][3];
      const float sum = e == 0 ? rsum[g2][0] : e == 1 ? rsum[g2][1] : e == 2 ? rsum[g2][2] : rsum[g2][3];
      const int row = 32 * hw + 16 * g2 + r15;
      const float mean = sum * (1.f / 512.f);
      float var = sq * (1.f / 512.f) - mean * mean;
      if (!(var > 1e-3f * mean * mean)) {
        const unsigned* xr = (const unsigned*)(xg + (size_t)(rg * 128 + row) * 1024);
        float acc = 0.f;
        for (int i = 0; i < 256; ++i) {
          const unsigned u = xr[i];
          const float a = bf_lo(u) - mean, b = bf_hi(u) - mean;
          acc = fmaf(a, a, fmaf(b, b, acc));
        }
        var = acc * (1.f / 512.f);
      }
      const float rstd = 1.0f / sqrtf(var + 1e-5f);
      *(f32x2*)(sm + K1_STATS + row * 8) = f32x2{rstd, -mean * rstd};
    }
  }
  WAIT_LGKM0();
  flag_add(sm, K1_FIN, lane);
  flag_wait(sm, K1_FIN, 4);
  STAMPT(ws, 1, 6);
  {
    const int p = lane & 7, lrow = cg * 8 + p;
    const f32x2 st = *(const f32x2*)(sm + K1_STATS + lrow * 8);
    const int grow = rg * 128 + lrow;
#pragma unroll
    for (int i = 0; i < 2; ++i) {
      const int c = (lane >> 3) + 8 * (2 * hw + i);
      const u32x4 u = xr2[i];
      f32x8 v;
#pragma unroll
      for (int e = 0; e < 4; ++e) { v[2 * e] = bf_lo(u[e]) * st[0] + st[1]; v[2 * e + 1] = bf_hi(u[e]) * st[0] + st[1]; }
      st16(ws_rsrc(ws), (unsigned)WS_XN + (((grow >> 5) * 32 + (c >> 1)) * 64 + (c & 1) * 32 + (grow & 31)) * 16,
           __builtin_convertvector(v, bf16x8));
    }
  }
  STAMPT(ws, 1, 7);
}

DI void k1_loader_wave(char* sm, unsigned sbase, const char* xg, const char* wqkv, const char* w1, const char* w2,
                       const char* wo, int lw, int lane, int bid, int rg, int cg, int rot, char* ws) {
  unsigned xoff[4], woff[3];
#pragma unroll
  for (int p = 0; p < 4; ++p) {
    int c = (lane & 7) ^ ((4 * p + (lane >> 4)) & 7);
    xoff[p] = (unsigned)((rg * 128 + 32 * lw + 8 * p + (lane >> 3)) * 1024 + c * 16);
  }
#pragma unroll
  for (int i = 0; i < 3; ++i) {
    int idx = 64 * (3 * lw + i) + lane;
    int row = idx / 12, ch = idx - row * 12;
    woff[i] = (unsigned)(row * 3072 + cg * 192 + ch * 16);
  }
  auto issue = [&](int s, int slot) {
    const int ks = (s + rot) & 7;
    const unsigned dst = sbase + slot;
#pragma unroll
    for (int i = 0; i < 4; ++i) glds16(xg + ks * 128, xoff[i], dst + lw * 4096 + i * 1024);
#pragma unroll
    for (int i = 0; i < 3; ++i) glds16(wqkv + (size_t)ks * 64 * 3072, woff[i], dst + K1_WOFF + lw * 3072 + i * 1024);
  };
  issue(0, 0);
  issue(1, K1_SLOT);
  const char* swsrc; int sld, kb, nb, sks; size_t sdst; bool perm;
  if (bid < 112) { swsrc = w1; sld = FF_ * 2; kb = bid / 14; nb = bid - kb * 14; sks = 32; sdst = WS_W1; perm = false; }
  else if (bid < 224) { int v = bid - 112; swsrc = w2; sld = D_ * 2; kb = v >> 2; nb = v & 3; sks = 112; sdst = WS_W2; perm = true; }
  else { int v = bid - 224; swsrc = wo; sld = D_ * 2; kb = v >> 2; nb = v & 3; sks = 32; sdst = WS_WO; perm = true; }
  const int h = lane >> 5, q = (lane & 15) >> 2;
  const unsigned cb = (16 * ((lane >> 4) & 1) + 4 * (lane & 3)) * 2;
  WAIT_LGKM0();
  BARRIER();
  {
    int islot = 2 * K1_SLOT;
#pragma nounroll
    for (int s = 2; s < 8; ++s) {
      wait_vm<7>(); flag_add(sm, K1_READY(s - 2), lane);
      if (s >= 5) flag_wait(sm, K1_DONE(s - 5), 8);
      issue(s, islot);
      islot += K1_SLOT;
      if (islot == K1_NSLOT * K1_SLOT) islot = 0;
    }
  }
  wait_vm<7>(); flag_add(sm, K1_READY(6), lane);
#pragma unroll
  for (int i = 0; i < 4; ++i) {
    const int p = 4 * lw + i;
    glds16(swsrc + (size_t)(kb * 64 + 4 * p) * sld + nb * 256, (unsigned)((lane >> 4) * sld + (lane & 15) * 16),
           sbase + K1_SIDE + p * 1024);
  }
  wait_vm<4>(); flag_add(sm, K1_READY(7), lane);
  WAIT_VM(0);
  {
    const int r0 = perm ? 4 * h : 8 * h, r1 = perm ? 8 + 4 * h : 8 * h + 4;
    const unsigned o0 = K1_SIDE + (16 * lw + r0 + q) * 256 + cb, o1 = K1_SIDE + (16 * lw + r1 + q) * 256 + cb;
#pragma unroll
    for (int nt = 0; nt < 4; ++nt) {
      bf16x8 f = cat8(lds_tr(sm, o0 + nt * 64), lds_tr(sm, o1 + nt * 64));
      st16(ws_rsrc(ws), (unsigned)sdst + ((nb * 4 + nt) * sks + kb * 4 + lw) * 1024 + lane * 16, f);
    }
  }
}

__global__ __launch_bounds__(768) void k1_qkv(const char* __restrict__ x, const char* __restrict__ wqkv,
                                              const char* __restrict__ w1, const char* __restrict__ w2,
                                              const char* __restrict__ wo, char* __restrict__ ws) {
  extern __shared__ __attribute__((aligned(16))) char sm[];
  const int lane = threadIdx.x & 63, wave = __builtin_amdgcn_readfirstlane(threadIdx.x >> 6);
  const int bid = blockIdx.x, xcd = bid & 7, idx = bid >> 3;
  const int rg = 4 * (xcd >> 1) + (idx & 3), cg = 8 * (xcd & 1) + (idx >> 2);
  if (threadIdx.x >= 256 && threadIdx.x < 272) *(unsigned*)(sm + K1_FLG + (threadIdx.x - 256) * 4) = 0u;
  if (wave >= 4) { WAIT_LGKM0(); BARRIER(); }
  if (wave < 4) {
    const int rot = ((idx >> 2) + 2 * (idx & 3)) & 7;
    k1_loader_wave(sm, lds_addr(sm), x, wqkv, w1, w2, wo, wave, lane, bid, rg, cg, rot, ws);
  } else if (wave < 8) {
    if (cg < 10) k1_gemm_wave<0>(sm, wave - 4, lane, rg, cg, ws);
    else if (cg == 10) k1_gemm_wave<1>(sm, wave - 4, lane, rg, cg, ws);
    else k1_gemm_wave<3>(sm, wave - 4, lane, rg, cg, ws);
  } else {
    if (bid == 0) st16(ws_rsrc(ws), (unsigned)WS_FLAG + (wave - 8) * 1024 + lane * 16, u32x4{0u, 0u, 0u, 0u});
#pragma unroll
    for (int i = 0; i < 2; ++i)
      st16(ws_rsrc(ws), (unsigned)WS_OF + bid * 8192 + (wave - 8) * 2048 + i * 1024 + lane * 16, u32x4{~0u, ~0u, ~0u, ~0u});
    k1_stats_wave(sm, x, wave - 8, lane, rg, cg, ws);
  }
}

constexpr int K2_FLG = KVHEAD;
constexpr int K2_LDS = K2_FLG + 64;
constexpr float SM_DELTA = 60.f;
typedef __attribute__((address_space(1))) unsigned gu32;
#define X_AUXB 16
#define SB() __builtin_amdgcn_sched_barrier(0)

DI float xhalf_max(float v) {
  auto r = __builtin_amdgcn_permlane32_swap(__float_as_uint(v), __float_as_uint(v), false, false);
  return fmaxf(__uint_as_float(r[0]), __uint_as_float(r[1]));
}
DI float max16(const f32x16& s) {
  float m01 = fmaxf(fmaxf(s[0], s[1]), s[2]), m23 = fmaxf(fmaxf(s[3], s[4]), s[5]);
  float m45 = fmaxf(fmaxf(s[6], s[7]), s[8]), m67 = fmaxf(fmaxf(s[9], s[10]), s[11]);
  float m89 = fmaxf(fmaxf(s[12], s[13]), s[14]);
  return fmaxf(fmaxf(fmaxf(m01, m23), m45), fmaxf(fmaxf(m67, m89), s[15]));
}
DI void lds_signal(char* sm, int word, int lane) { flag_add(sm, K2_FLG + 4 * word, lane); }
DI void lds_wait(char* sm, int word, unsigned target) { flag_wait(sm, K2_FLG + 4 * word, target); }

template <int NKS, int PF, int AUXB>
DI void dgemm64_ring(__amdgpu_buffer_rsrc_t rs, unsigned a0, unsigned a1, unsigned b0, unsigned b1, int rot, f32x16 (&acc)[4]) {
  u32x4 A0[PF], A1[PF], B0[PF], B1[PF];
  int ko = rot * 1024;
#pragma unroll
  for (int kk = 0; kk < NKS + PF - 1; ++kk) {
    if (kk < NKS) {
      const int sl = kk % PF;
      A0[sl] = __builtin_amdgcn_raw_buffer_load_b128(rs, a0, ko, 0);
      B0[sl] = __builtin_amdgcn_raw_buffer_load_b128(rs, b0, ko, AUXB);
      A1[sl] = __builtin_amdgcn_raw_buffer_load_b128(rs, a1, ko, 0);
      B1[sl] = __builtin_amdgcn_raw_buffer_load_b128(rs, b1, ko, AUXB);
      ko += 1024;
      if (ko == NKS * 1024) ko = 0;
    }
    SB();
    if (kk >= PF - 1) {
      const int sl = (kk - (PF - 1)) % PF;
      const bf16x8 fa0 = __builtin_bit_cast(bf16x8, A0[sl]), fa1 = __builtin_bit_cast(bf16x8, A1[sl]);
      const bf16x8 fb0 = __builtin_bit_cast(bf16x8, B0[sl]), fb1 = __builtin_bit_cast(bf16x8, B1[sl]);
#define X_GAP
      acc[0] = MFMA(fa0, fb0, acc[0]);
      SB(); X_GAP; SB();
      acc[2] = MFMA(fa1, fb0, acc[2]);
      SB(); X_GAP; SB();
      acc[1] = MFMA(fa0, fb1, acc[1]);
      SB(); X_GAP; SB();
      acc[3] = MFMA(fa1, fb1, acc[3]);
    }
    SB();
  }
  asm volatile("" : "+v"(acc[0]), "+v"(acc[1]), "+v"(acc[2]), "+v"(acc[3]));
}

DI void k2_ffn_wave(char* sm, const char* __restrict__ x, char* __restrict__ ws, char* __restrict__ out, int w, int lane, int panel, int ci, int head) {
  {
    const unsigned sb = lds_addr(sm) + w * 1024, dl = lane * 16;
    const char* kvg = ws + WS_KV + (size_t)head * KVHEAD + w * 1024;
#pragma unroll
    for (int i = 0; i < 8; ++i) glds16(kvg + i * 4096, dl, sb + i * 4096);
    WAIT_LGKM0();
    BARRIER();
    WAIT_VM(4);
    lds_signal(sm, 0, lane);
#pragma unroll
    for (int i = 8; i < 16; ++i) glds16(kvg + i * 4096, dl, sb + i * 4096);
    WAIT_VM(4);
    lds_signal(sm, 1, lane);
#pragma unroll
    for (int i = 16; i < 34; ++i) glds16(kvg + i * 4096, dl, sb + i * 4096);
    WAIT_VM(0);
    lds_signal(sm, 2, lane);
  }
#define X_FPRIO 2
  __builtin_amdgcn_s_setprio(X_FPRIO);
  const __amdgpu_buffer_rsrc_t rs = __builtin_amdgcn_make_buffer_rsrc((void*)ws, 0, (int)WS_END, 0x00020000);
  gu32* flags = (gu32*)(ws + WS_FLAG) + panel * 128;
  STAMPT(ws, 2, 0);
  if (ci < 28) {
    const int rt0 = panel * 8 + 2 * w, nt0 = 2 * ci;
    const unsigned a0 = (unsigned)WS_W1 + nt0 * 32768 + lane * 16, b0 = (unsigned)WS_XN + rt0 * 32768 + lane * 16;
    f32x16 acc[4] = {};
#define X_ROT1 ci
    dgemm64_ring<32, 5, 0>(rs, a0, a0 + 32768, b0, b0 + 32768, X_ROT1, acc);
    STAMPT(ws, 2, 5);
#pragma unroll
    for (int i = 0; i < 2; ++i)
#pragma unroll
      for (int j = 0; j < 2; ++j) {
        f32x16 v = acc[2 * i + j];
#pragma unroll
        for (int e = 0; e < 16; ++e) v[e] = fmaxf(v[e], 0.f);
        const unsigned dst = (unsigned)WS_HF + ((rt0 + j) * 112 + (nt0 + i) * 2) * 1024 + lane * 16;
        __builtin_amdgcn_raw_buffer_store_b128(__builtin_bit_cast(u32x4, pack_lo(v)), rs, dst, 0, 16);
        __builtin_amdgcn_raw_buffer_store_b128(__builtin_bit_cast(u32x4, pack_hi(v)), rs, dst + 1024, 0, 16);
      }
    WAIT_VM(0);
    if (lane == 0) __hip_atomic_store(flags + w * 32 + ci, 1u, __ATOMIC_RELAXED, __HIP_MEMORY_SCOPE_AGENT);
  }
  STAMPT(ws, 2, 1);
  const int rp2 = ci & 3, np2 = ci >> 2;
  {
    __builtin_amdgcn_s_setprio(0);
    gu32* f = flags + rp2 * 32 + 7 * w + min(lane, 6);
    for (unsigned spins = 0; spins < (1u << 20); ++spins) {
      unsigned v = __hip_atomic_load(f, __ATOMIC_RELAXED, __HIP_MEMORY_SCOPE_AGENT);
      if (__all(v == 1u)) break;
      __builtin_amdgcn_s_sleep(8);
    }
    __builtin_amdgcn_fence(__ATOMIC_ACQUIRE, "wavefront");
    asm volatile("" ::: "memory");
  }
  STAMPT(ws, 2, 2);
  __builtin_amdgcn_s_setprio(X_FPRIO);
  const int rt0 = panel * 8 + 2 * rp2, nt0 = 2 * np2;
  f32x16 acc[4] = {};
  {
    const unsigned a0 = (unsigned)WS_W2 + (nt0 * 112 + 28 * w) * 1024 + lane * 16;
    const unsigned b0 = (unsigned)WS_HF + (rt0 * 112 + 28 * w) * 1024 + lane * 16;
    dgemm64_ring<28, 5, X_AUXB>(rs, a0, a0 + 112 * 1024, b0, b0 + 112 * 1024, (7 * rp2 + 3 * np2) % 28, acc);
  }
  STAMPT(ws, 2, 3);
  const int h = lane >> 5, r31 = lane & 31;
  const size_t eoff = ((size_t)((rt0 + (w & 1)) * 32 + r31) * D_ + (nt0 + (w >> 1)) * 32 + 4 * h) * 2;
  u32x2 xv[4];
#pragma unroll
  for (int g = 0; g < 4; ++g) xv[g] = *(const u32x2*)(x + eoff + 16 * g);
  __builtin_amdgcn_s_setprio(0);
  flag_wait<6>(sm, K2_FLG + 12, 8);
  {
    const int wl = 65536 + w * 16384;
    const char* wog = ws + WS_WO + (size_t)(nt0 * 32 + 8 * w) * 1024;
#pragma unroll
    for (int i = 0; i < 16; ++i) glds16(wog + (i >> 3) * 32768 + (i & 7) * 1024, lane * 16, lds_addr(sm) + wl + i * 1024);
    gu32* f = (gu32*)(ws + WS_OF + ((rt0 + ((lane >> 3) & 1)) * 32 + 8 * w + (lane & 7)) * 1024 + 1020);
    for (unsigned spins = 0; spins < (1u << 20); ++spins) {
      unsigned v = __hip_atomic_load(f, __ATOMIC_RELAXED, __HIP_MEMORY_SCOPE_AGENT);
      if (__all(v != ~0u)) break;
      __builtin_amdgcn_s_sleep(2);
    }
    asm volatile("" ::: "memory");
    STAMPT(ws, 2, 6);
    const unsigned b0 = (unsigned)WS_OF + (rt0 * 32 + 8 * w) * 1024 + lane * 16;
    u32x4 B[16];
    for (unsigned spins = 0; spins < (1u << 16); ++spins) {
      asm volatile("" ::: "memory");
#pragma unroll
      for (int i = 0; i < 16; ++i) B[i] = __builtin_amdgcn_raw_buffer_load_b128(rs, b0, (i & 1) * 32768 + (i >> 1) * 1024, 16);
      unsigned m = 0;
#pragma unroll
      for (int i = 0; i < 16; ++i) m = max(max(m, B[i][0]), max(B[i][1], max(B[i][2], B[i][3])));
      if (__builtin_expect(!__any(m == ~0u), 1)) break;
    }
    WAIT_VM(0);
#pragma unroll
    for (int ks = 0; ks < 8; ++ks) {
      const bf16x8 fa0 = lds_b128(sm, wl + ks * 1024 + lane * 16), fa1 = lds_b128(sm, wl + 8192 + ks * 1024 + lane * 16);
      const bf16x8 fb0 = __builtin_bit_cast(bf16x8, B[2 * ks]), fb1 = __builtin_bit_cast(bf16x8, B[2 * ks + 1]);
      acc[0] = MFMA(fa0, fb0, acc[0]);
      acc[2] = MFMA(fa1, fb0, acc[2]);
      acc[1] = MFMA(fa0, fb1, acc[1]);
      acc[3] = MFMA(fa1, fb1, acc[3]);
    }
  }
  {
    f32x4* red = (f32x4*)sm + lane;
#pragma unroll
    for (int t = 0; t < 4; ++t)
#pragma unroll
      for (int g = 0; g < 4; ++g)
        red[((t * 4 + w) * 4 + g) * 64] = f32x4{acc[t][4 * g], acc[t][4 * g + 1], acc[t][4 * g + 2], acc[t][4 * g + 3]};
    WAIT_LGKM0();
    lds_signal(sm, 4, lane);
    lds_wait(sm, 4, 4);
#pragma unroll
    for (int g = 0; g < 4; ++g) {
      f32x4 v = red[((w * 4 + 0) * 4 + g) * 64];
#pragma unroll
      for (int src = 1; src < 4; ++src) v += red[((w * 4 + src) * 4 + g) * 64];
      v[0] += bf_lo(xv[g][0]); v[1] += bf_hi(xv[g][0]); v[2] += bf_lo(xv[g][1]); v[3] += bf_hi(xv[g][1]);
      *(bf16x4*)(out + eoff + 16 * g) = __builtin_convertvector(v, bf16x4);
    }
  }
  STAMPT(ws, 2, 4);
}

__global__ __launch_bounds__(768) void k2_attn(const char* __restrict__ x, char* __restrict__ ws, char* __restrict__ out) {
  extern __shared__ __attribute__((aligned(16))) char sm[];
  const int lane = threadIdx.x & 63, wave = __builtin_amdgcn_readfirstlane(threadIdx.x >> 6);
  const int bid = blockIdx.x, xcd = bid & 7, idx = bid >> 3;
  if (threadIdx.x < 16) *(unsigned*)(sm + K2_FLG + threadIdx.x * 4) = 0u;
  const int head = xcd * 4 + (idx >> 3), qb = idx & 7, rt = qb * 8 + wave;
  if (wave >= 8) {
    k2_ffn_wave(sm, x, ws, out, wave - 8, lane, xcd, idx, head);
    return;
  }
  STAMPT(ws, 2, 0);
  const bf16x8 qf = *((const bf16x8*)(ws + WS_QF) + ((size_t)(head * 64 + rt) * 64 + lane));
  const int h = lane >> 5, r31 = lane & 31;
  const unsigned koff = lane * 16;
  const unsigned voff = 1024 + ((r31 < 16) ? vslot(r31, h) : 15) * 16;
  WAIT_LGKM0();
  BARRIER();
  f32x16 negm = {}, O = {};
#define EXP(v, i) v[i] = __builtin_amdgcn_exp2f(v[i])
#define TILE_STEP(SC, SN, OFFK2, OFFV0N, OFFV1)                                   \
  {                                                                               \
    SN = MFMA(kn, qf, negm);                                                      \
    SB();                                                                         \
    kn = lds_b128(kp, OFFK2);                                                     \
    EXP(SC, 0); EXP(SC, 1); EXP(SC, 2); EXP(SC, 3); EXP(SC, 4); EXP(SC, 5);       \
    SB();                                                                         \
    O = MFMA(v1p, p1p, O);                                                        \
    SB();                                                                         \
    v1p = lds_b128(vp, OFFV1);                                                    \
    EXP(SC, 6); EXP(SC, 7);                                                       \
    bf16x8 p0 = pack_lo(SC);                                                      \
    SB();                                                                         \
    EXP(SC, 8); EXP(SC, 9);                                                       \
    SB();                                                                         \
    O = MFMA(v0, p0, O);                                                          \
    SB();                                                                         \
    v0 = lds_b128(vp, OFFV0N);                                                    \
    EXP(SC, 10); EXP(SC, 11); EXP(SC, 12); EXP(SC, 13); EXP(SC, 14); EXP(SC, 15); \
    p1p = pack_hi(SC);                                                            \
    SB();                                                                         \
  }
#define TILE4()                                                        \
  TILE_STEP(sA, sB, 2 * KVTILE, 1 * KVTILE, 0 * KVTILE + VBLK);        \
  TILE_STEP(sB, sA, 3 * KVTILE, 2 * KVTILE, 1 * KVTILE + VBLK);        \
  TILE_STEP(sA, sB, 4 * KVTILE, 3 * KVTILE, 2 * KVTILE + VBLK);        \
  TILE_STEP(sB, sA, 5 * KVTILE, 4 * KVTILE, 3 * KVTILE + VBLK);        \
  kp += 4 * KVTILE; vp += 4 * KVTILE;
  {
    const char* kp = sm + koff;
    const char* vp = sm + voff;
#define X_SW 20
    lds_wait(sm, 0, 4);
    STAMPT(ws, 2, 1);
    const bf16x8 k0 = lds_b128(kp, 0);
    bf16x8 kn = lds_b128(kp, KVTILE), v0 = lds_b128(vp, 0), v1p = lds_b128(vp, VBLK), p1p = {};
    {
      const f32x16 z = {};
      f32x16 s0 = MFMA(k0, qf, z), s1 = MFMA(kn, qf, z);
      const float nm = -(xhalf_max(fmaxf(max16(s0), max16(s1))) + SM_DELTA);
#pragma unroll
      for (int e = 0; e < 16; ++e) negm[e] = nm;
    }
    f32x16 sA = MFMA(k0, qf, negm), sB;
    int ev = 4;
    for (int t = 0; t < 64; t += 4) {
      if (__builtin_expect(t == ev, 0)) {
        lds_wait(sm, t == 4 ? 1 : 2, 4);
        if (t != 4 && wave >= 4) __builtin_amdgcn_s_setprio(1);
        ev = (t == 4) ? X_SW : -100;
      }
      TILE4();
      asm volatile("" : "+v"(O), "+v"(sA), "+v"(p1p));
    }
    O = MFMA(v1p, p1p, O);
  }
  STAMPT(ws, 2, 2);
  float l = O[8];
  bool ok = (l < 0x1p100f);
#pragma unroll
  for (int e = 0; e < 8; ++e) ok = ok && (fabsf(O[e]) < 0x1p126f);
  if (!__all(ok)) {
    float m = -INFINITY;
#pragma unroll
    for (int e = 0; e < 16; ++e) O[e] = 0.f;
    for (int t = 0; t < 64; ++t) {
      f32x16 z = {};
      f32x16 sc = MFMA(lds_b128(sm, koff + t * KVTILE), qf, z);
      bf16x8 v0 = lds_b128(sm, voff + t * KVTILE), v1 = lds_b128(sm, voff + t * KVTILE + VBLK);
      float mn = fmaxf(m, xhalf_max(max16(sc)));
      float alpha = __builtin_amdgcn_exp2f(m - mn);
      m = mn;
#pragma unroll
      for (int e = 0; e < 16; ++e) { O[e] *= alpha; sc[e] = __builtin_amdgcn_exp2f(sc[e] - mn); }
      O = MFMA(v0, pack_lo(sc), O);
      O = MFMA(v1, pack_hi(sc), O);
    }
    l = O[8];
  }
  const float rl = 1.0f / l;
#pragma unroll
  for (int e = 0; e < 8; ++e) O[e] *= rl;
  u32x4 po = __builtin_bit_cast(u32x4, pack_lo(O));
#pragma unroll
  for (int e = 0; e < 4; ++e) po[e] = min(po[e], 0xFFFFFFFEu);
  __builtin_amdgcn_raw_buffer_store_b128(po, ws_rsrc(ws), (unsigned)WS_OF + (rt * 32 + head) * 1024 + lane * 16, 0, 16);
  WAIT_LGKM0();
  lds_signal(sm, 3, lane);
  STAMPT(ws, 2, 3);
}

extern "C" void kernel_launch(void* const* d_in, const int* in_sizes, int n_in, void* d_out, int out_size, void* d_ws,
                              size_t ws_size, hipStream_t stream) {
  const char* x = (const char*)d_in[0];
  const char* wqkv = (const char*)d_in[1];
  const char* wo = (const char*)d_in[2];
  const char* w1 = (const char*)d_in[4];
  const char* w2 = (const char*)d_in[5];
  char* ws = (char*)d_ws;
  (void)hipFuncSetAttribute((const void*)k1_qkv, hipFuncAttributeMaxDynamicSharedMemorySize, K1_LDS);
  (void)hipFuncSetAttribute((const void*)k2_attn, hipFuncAttributeMaxDynamicSharedMemorySize, K2_LDS);
  hipLaunchKernelGGL(k1_qkv, dim3(256), dim3(768), K1_LDS, stream, x, wqkv, w1, w2, wo, ws);
  hipLaunchKernelGGL(k2_attn, dim3(256), dim3(768), K2_LDS, stream, x, ws, (char*)d_out);
}
```

```cpp
#include <hip/hip_runtime.h>
#include <hip/hip_bf16.h>
#include <stdint.h>

typedef __attribute__((ext_vector_type(8))) __bf16 bf16x8;
typedef __attribute__((ext_vector_type(4))) __bf16 bf16x4;
typedef __attribute__((ext_vector_type(16))) float f32x16;
typedef __attribute__((ext_vector_type(8))) float f32x8;
typedef __attribute__((ext_vector_type(4))) float f32x4;
typedef __attribute__((ext_vector_type(2))) float f32x2;
typedef __attribute__((ext_vector_type(4))) unsigned u32x4;
typedef __attribute__((ext_vector_type(2))) unsigned u32x2;
#define LDS_AS __attribute__((address_space(3)))
#define DI __device__ __forceinline__
#define MFMA(a, b, c) __builtin_amdgcn_mfma_f32_32x32x16_bf16((a), (b), (c), 0, 0, 0)

constexpr int S_ = 2048, D_ = 512, FF_ = 1792, NH_ = 32;
constexpr int KVTILE = 2112;
constexpr int VBLK = 544;
constexpr int KVHEAD = 136 * 1024;
constexpr size_t WS_QF = 0;
constexpr size_t WS_KV = WS_QF + (size_t)2 * 1024 * 1024;
constexpr size_t WS_XN = WS_KV + (size_t)NH_ * KVHEAD;
constexpr size_t WS_W1 = WS_XN + (size_t)2 * 1024 * 1024;
constexpr size_t WS_W2 = WS_W1 + (size_t)1792 * 1024;
constexpr size_t WS_WO = WS_W2 + (size_t)1792 * 1024;
constexpr size_t WS_HF = WS_WO + (size_t)512 * 1024;
constexpr size_t WS_OF = WS_HF + (size_t)64 * 112 * 1024;
constexpr size_t WS_Y = WS_OF + (size_t)2 * 1024 * 1024;
constexpr size_t WS_FLAG = WS_Y + (size_t)4 * 1024 * 1024;
constexpr size_t WS_FLAG2 = WS_FLAG + 4096;
constexpr size_t WS_END = WS_FLAG2 + 8192;

#define STAMPT(ws, kid, i)
constexpr float QSCALE = 0.25f * 1.4426950408889634f;

DI unsigned lds_addr(const void* p) { return (unsigned)(uintptr_t)(LDS_AS const char*)p; }
DI void glds16(const void* sbase, unsigned voff, unsigned ldst) {
  unsigned keep;
  asm volatile("s_mov_b32 %0, m0\n\ts_mov_b32 m0, %3\n\ts_nop 0\n\tglobal_load_lds_dwordx4 %1, %2\n\ts_mov_b32 m0, %0"
               : "=&s"(keep) : "v"(voff), "s"(sbase), "s"(ldst) : "memory");
}
#define WAIT_VM(n) asm volatile("s_waitcnt vmcnt(" #n ")" ::: "memory")
#define WAIT_LGKM0() asm volatile("s_waitcnt lgkmcnt(0)" ::: "memory")
#define BARRIER() __builtin_amdgcn_s_barrier()
#define TBAR(acc) __builtin_amdgcn_s_barrier()
#define TVM(acc, stmt) stmt
#define TBAR_OUT(ws, acc, i)
template <int N> DI void wait_vm() { asm volatile("s_waitcnt vmcnt(%0)" ::"i"(N) : "memory"); }

#define X_STAUX 16
DI __amdgpu_buffer_rsrc_t ws_rsrc(const void* ws) { return __builtin_amdgcn_make_buffer_rsrc((void*)ws, 0, (int)WS_END, 0x00020000); }
template <typename T> DI void st16(__amdgpu_buffer_rsrc_t rs, unsigned off, T v) {
  __builtin_amdgcn_raw_buffer_store_b128(__builtin_bit_cast(u32x4, v), rs, off, 0, X_STAUX);
}
DI bf16x8 lds_b128(const char* sm, unsigned off) { return *(const bf16x8*)(sm + off); }
DI bf16x4 lds_tr(const char* sm, unsigned off) {
  return __builtin_amdgcn_ds_read_tr16_b64_v4bf16((LDS_AS bf16x4*)(LDS_AS char*)(sm + off));
}
DI bf16x8 cat8(bf16x4 lo, bf16x4 hi) { return __builtin_shufflevector(lo, hi, 0, 1, 2, 3, 4, 5, 6, 7); }
DI bf16x8 pack_lo(const f32x16& v) { return __builtin_convertvector(__builtin_shufflevector(v, v, 0, 1, 2, 3, 4, 5, 6, 7), bf16x8); }
DI bf16x8 pack_hi(const f32x16& v) { return __builtin_convertvector(__builtin_shufflevector(v, v, 8, 9, 10, 11, 12, 13, 14, 15), bf16x8); }
DI float bf_lo(unsigned u) { return __uint_as_float(u << 16); }
DI float bf_hi(unsigned u) { return __uint_as_float(u & 0xffff0000u); }
DI int vslot(int d, int h) {
  int s = (d < 4) ? d : (d >= 12) ? d - 8 : (d < 11) ? d + 4 : -1;
  return (s >= 0) ? (16 * h + s) : (32 + h);
}

DI LDS_AS unsigned* lds_word(char* sm, int off) { return (LDS_AS unsigned*)(LDS_AS char*)(sm + off); }
DI void flag_add(char* sm, int off, int lane) {
  if (lane == 0) __hip_atomic_fetch_add(lds_word(sm, off), 1u, __ATOMIC_RELAXED, __HIP_MEMORY_SCOPE_WORKGROUP);
  asm volatile("" ::: "memory");
}
DI unsigned flag_peek(char* sm, int off) { return __hip_atomic_load(lds_word(sm, off), __ATOMIC_RELAXED, __HIP_MEMORY_SCOPE_WORKGROUP); }
template <int SLEEP = 1>
DI void flag_wait(char* sm, int off, unsigned target) {
  while ((unsigned)__builtin_amdgcn_readfirstlane(flag_peek(sm, off)) < target) __builtin_amdgcn_s_sleep(SLEEP);
  asm volatile("" ::: "memory");
}

constexpr int K1_SLOT = 28672, K1_WOFF = 16384, K1_NSLOT = 5;
constexpr int K1_STATS = K1_SLOT * K1_NSLOT;
constexpr int K1_WPART = K1_STATS + 1024;
constexpr int K1_FLG = K1_WPART + 768;
constexpr int K1_SIDE = K1_FLG + 256;
constexpr int K1_SCR = K1_SIDE + 16384;
constexpr int K1_LDS = K1_SCR + 256;
#define K1_READY(s) (K1_FLG + 4 * (s))
#define K1_DONE(s) (K1_FLG + 32 + 4 * (s))
#define K1_FIN (K1_FLG + 44)

template <int NV>
DI void k1_gemm_wave(char* sm, int w, int lane, int rg, int cg, char* ws) {
  const int h = lane >> 5, r31 = lane & 31;
  STAMPT(ws, 1, 0);
  unsigned aoff[4];
  {
    const int row = 32 * w + r31;
#pragma unroll
    for (int ks = 0; ks < 4; ++ks) aoff[ks] = row * 128 + (((2 * ks + h) ^ ((row >> 1) & 7)) << 4);
  }
  const unsigned boff = K1_WOFF + (8 * h + ((lane & 15) >> 2)) * 192 + (16 * ((lane >> 4) & 1) + 4 * (lane & 3)) * 2;
  f32x16 acc[3] = {};
  bf16x8 fa[4], fb[4][3];
#pragma unroll
  for (int i = 2; i < 4; ++i) {
    fa[i] = bf16x8{};
#pragma unroll
    for (int j = 0; j < 3; ++j) fb[i][j] = bf16x8{};
  }
#define K1_MMA(ps)                                                        \
  _Pragma("unroll") for (int j = 0; j < 3; ++j) {                         \
    if (j >= 3 - NV) acc[j] = MFMA(fa[ps], fb[ps][j], acc[j]);   \
    else acc[j] = MFMA(fb[ps][j], fa[ps], acc[j]);               \
  }
  int slot = 0;
  unsigned rdy = 0;
#pragma nounroll
  for (int s = 0; s < 8; ++s) {
    if ((unsigned)__builtin_amdgcn_readfirstlane(rdy) < 4u) flag_wait(sm, K1_READY(s), 4);
    asm volatile("" ::: "memory");
    if (s == 0) STAMPT(ws, 1, 1);
    if (s == 4) STAMPT(ws, 1, 3);
    const char* sl = sm + slot;
#pragma unroll
    for (int ks = 0; ks < 4; ++ks) {
      if (ks == 2) rdy = flag_peek(sm, K1_READY(s + 1));
      fa[ks] = lds_b128(sl, aoff[ks]);
#pragma unroll
      for (int j = 0; j < 3; ++j)
        fb[ks][j] = cat8(lds_tr(sl, boff + ks * 16 * 192 + 64 * j), lds_tr(sl, boff + (ks * 16 + 4) * 192 + 64 * j));
      __builtin_amdgcn_sched_barrier(0);
      K1_MMA((ks + 2) & 3);
      __builtin_amdgcn_sched_barrier(0);
      if (ks == 1 && s >= 1 && s <= 3) flag_add(sm, K1_DONE(s - 1), lane);
    }
    slot += K1_SLOT;
    if (slot == K1_NSLOT * K1_SLOT) slot = 0;
  }
  K1_MMA(2);
  K1_MMA(3);
  asm volatile("" : "+v"(acc[0]), "+v"(acc[1]), "+v"(acc[2]));
  STAMPT(ws, 1, 5);
  flag_wait(sm, K1_FIN, 4);
  STAMPT(ws, 1, 6);
  const int rt = rg * 4 + w;
  const __amdgpu_buffer_rsrc_t rs = ws_rsrc(ws);
  const f32x2* stats = (const f32x2*)(sm + K1_STATS) + 32 * w;
  const float* wsum = (const float*)(sm + K1_WPART);
#pragma unroll
  for (int j = 0; j < 3; ++j) {
    const int ct = cg * 3 + j, role = ct >> 4, hp = (ct & 15) * 2;
    f32x16 v;
    if (j >= 3 - NV) {
      const float wsn = wsum[32 * j + r31] + wsum[96 + 32 * j + r31];
#pragma unroll
      for (int g = 0; g < 4; ++g) {
        f32x4 s0 = *(const f32x4*)(stats + 8 * g + 4 * h), s1 = *(const f32x4*)(stats + 8 * g + 4 * h + 2);
        v[4 * g + 0] = acc[j][4 * g + 0] * s0[0] + s0[1] * wsn;
        v[4 * g + 1] = acc[j][4 * g + 1] * s0[2] + s0[3] * wsn;
        v[4 * g + 2] = acc[j][4 * g + 2] * s1[0] + s1[1] * wsn;
        v[4 * g + 3] = acc[j][4 * g + 3] * s1[2] + s1[3] * wsn;
      }
      const unsigned base = (unsigned)WS_KV + (hp + (r31 >> 4)) * KVHEAD + rt * KVTILE + 1024;
      const int sl = vslot(r31 & 15, h);
      st16(rs, base + sl * 16, pack_lo(v));
      st16(rs, base + VBLK + sl * 16, pack_hi(v));
      if ((lane & 15) == 0)
        st16(rs, base + h * VBLK + 15 * 16, u32x4{0x3f803f80u, 0x3f803f80u, 0x3f803f80u, 0x3f803f80u});
    } else {
      f32x2 st = stats[r31];
      if (role == 0) { st[0] *= QSCALE; st[1] *= QSCALE; }
#pragma unroll
      for (int g = 0; g < 4; ++g) {
        f32x4 wsv = *(const f32x4*)(wsum + 32 * j + 8 * g + 4 * h) + *(const f32x4*)(wsum + 96 + 32 * j + 8 * g + 4 * h);
#pragma unroll
        for (int e = 0; e < 4; ++e) v[4 * g + e] = acc[j][4 * g + e] * st[0] + st[1] * wsv[e];
      }
      if (role == 0) {
        const unsigned base = (unsigned)WS_QF + (hp * 64 + rt) * 1024 + lane * 16;
        st16(rs, base, pack_lo(v));
        st16(rs, base + 64 * 1024, pack_hi(v));
      } else {
        const unsigned base = (unsigned)WS_KV + hp * KVHEAD + rt * KVTILE + lane * 16;
        st16(rs, base, pack_lo(v));
        st16(rs, base + KVHEAD, pack_hi(v));
      }
    }
  }
  STAMPT(ws, 1, 7);
}

#define MFMA16(a, b, c) __builtin_amdgcn_mfma_f32_16x16x32_bf16((a), (b), (c), 0, 0, 0)
DI void k1_stats_wave(char* sm, const char* __restrict__ xg, int hw, int lane, int rg, int cg, char* ws) {
  STAMPT(ws, 1, 0);
  const int r15 = lane & 15, kq = lane >> 4;
  unsigned aoff[4];
#pragma unroll
  for (int i = 0; i < 4; ++i) {
    const int row = 32 * hw + 16 * (i >> 1) + r15, c = 4 * (i & 1) + kq;
    aoff[i] = row * 128 + ((c ^ ((row >> 1) & 7)) << 4);
  }
  const unsigned boff = K1_WOFF + (32 * (hw & 1) + 8 * kq + (r15 >> 2)) * 192 + (48 * (hw >> 1) + 4 * (lane & 3)) * 2;
  const bf16x8 ones = __builtin_bit_cast(bf16x8, u32x4{0x3f803f80u, 0x3f803f80u, 0x3f803f80u, 0x3f803f80u});
  f32x4 gram[2] = {}, rsum[2] = {}, wacc[3] = {};
  bf16x8 xa[2][4] = {}, wb[2][3] = {};
  int slot = 0;
  u32x4 xr2[2];
#pragma unroll
  for (int i = 0; i < 2; ++i)
    xr2[i] = *(const u32x4*)(xg + (size_t)(rg * 128 + cg * 8 + (lane & 7)) * 1024 + ((lane >> 3) + 8 * (2 * hw + i)) * 16);
  auto math = [&](const bf16x8 (&xp)[4], const bf16x8 (&wp)[3]) {
#pragma unroll
    for (int i = 0; i < 4; ++i) {
      gram[i >> 1] = MFMA16(xp[i], xp[i], gram[i >> 1]);
      rsum[i >> 1] = MFMA16(xp[i], ones, rsum[i >> 1]);
    }
#pragma unroll
    for (int j = 0; j < 3; ++j) wacc[j] = MFMA16(ones, wp[j], wacc[j]);
  };
#pragma nounroll
  for (int s2 = 0; s2 < 4; ++s2) {
#pragma unroll
    for (int par = 0; par < 2; ++par) {
      flag_wait(sm, K1_READY(2 * s2 + par), 4);
      const char* sl = sm + slot;
#pragma unroll
      for (int i = 0; i < 4; ++i) xa[par][i] = lds_b128(sl, aoff[i]);
#pragma unroll
      for (int j = 0; j < 3; ++j) wb[par][j] = cat8(lds_tr(sl, boff + 32 * j), lds_tr(sl, boff + 4 * 192 + 32 * j));
      __builtin_amdgcn_sched_barrier(0);
      math(xa[par ^ 1], wb[par ^ 1]);
      __builtin_amdgcn_sched_barrier(0);
      WAIT_LGKM0();
      if (2 * s2 + par <= 2) flag_add(sm, K1_DONE(2 * s2 + par), lane);
      slot += K1_SLOT;
      if (slot == K1_NSLOT * K1_SLOT) slot = 0;
    }
  }
  math(xa[1], wb[1]);
  STAMPT(ws, 1, 5);
  if (lane < 16) {
#pragma unroll
    for (int j = 0; j < 3; ++j) *(float*)(sm + K1_WPART + ((hw & 1) * 96 + 48 * (hw >> 1) + 16 * j + lane) * 4) = wacc[j][0];
  }
  if ((r15 >> 2) == kq) {
#pragma unroll
    for (int g2 = 0; g2 < 2; ++g2) {
      const int e = r15 & 3;
      const float sq = e == 0 ? gram[g2][0] : e == 1 ? gram[g2][1] : e == 2 ? gram[g2][2] : gram[g2][3];
      const float sum = e == 0 ? rsum[g2][0] : e == 1 ? rsum[g2][1] : e == 2 ? rsum[g2][2] : rsum[g2][3];
      const int row = 32 * hw + 16 * g2 + r15;
      const float mean = sum * (1.f / 512.f);
      float var = sq * (1.f / 512.f) - mean * mean;
      if (!(var > 1e-3f * mean * mean)) {
        const unsigned* xr = (const unsigned*)(xg + (size_t)(rg * 128 + row) * 1024);
        float acc = 0.f;
        for (int i = 0; i < 256; ++i) {
          const unsigned u = xr[i];
          const float a = bf_lo(u) - mean, b = bf_hi(u) - mean;
          acc = fmaf(a, a, fmaf(b, b, acc));
        }
        var = acc * (1.f / 512.f);
      }
      const float rstd = 1.0f / sqrtf(var + 1e-5f);
      *(f32x2*)(sm + K1_STATS + row * 8) = f32x2{rstd, -mean * rstd};
    }
  }
  WAIT_LGKM0();
  flag_add(sm, K1_FIN, lane);
  flag_wait(sm, K1_FIN, 4);
  STAMPT(ws, 1, 6);
  {
    const int p = lane & 7, lrow = cg * 8 + p;
    const f32x2 st = *(const f32x2*)(sm + K1_STATS + lrow * 8);
    const int grow = rg * 128 + lrow;
#pragma unroll
    for (int i = 0; i < 2; ++i) {
      const int c = (lane >> 3) + 8 * (2 * hw + i);
      const u32x4 u = xr2[i];
      f32x8 v;
#pragma unroll
      for (int e = 0; e < 4; ++e) { v[2 * e] = bf_lo(u[e]) * st[0] + st[1]; v[2 * e + 1] = bf_hi(u[e]) * st[0] + st[1]; }
      st16(ws_rsrc(ws), (unsigned)WS_XN + (((grow >> 5) * 32 + (c >> 1)) * 64 + (c & 1) * 32 + (grow & 31)) * 16,
           __builtin_convertvector(v, bf16x8));
    }
  }
  STAMPT(ws, 1, 7);
}

DI void k1_loader_wave(char* sm, unsigned sbase, const char* xg, const char* wqkv, const char* w1, const char* w2,
                       const char* wo, int lw, int lane, int bid, int rg, int cg, int rot, char* ws) {
  unsigned xoff[4], woff[3];
#pragma unroll
  for (int p = 0; p < 4; ++p) {
    int c = (lane & 7) ^ ((4 * p + (lane >> 4)) & 7);
    xoff[p] = (unsigned)((rg * 128 + 32 * lw + 8 * p + (lane >> 3)) * 1024 + c * 16);
  }
#pragma unroll
  for (int i = 0; i < 3; ++i) {
    int idx = 64 * (3 * lw + i) + lane;
    int row = idx / 12, ch = idx - row * 12;
    woff[i] = (unsigned)(row * 3072 + cg * 192 + ch * 16);
  }
  auto issue = [&](int s, int slot) {
    const int ks = (s + rot) & 7;
    const unsigned dst = sbase + slot;
#pragma unroll
    for (int i = 0; i < 4; ++i) glds16(xg + ks * 128, xoff[i], dst + lw * 4096 + i * 1024);
#pragma unroll
    for (int i = 0; i < 3; ++i) glds16(wqkv + (size_t)ks * 64 * 3072, woff[i], dst + K1_WOFF + lw * 3072 + i * 1024);
  };
  issue(0, 0);
  issue(1, K1_SLOT);
  const char* swsrc; int sld, kb, nb, sks; size_t sdst; bool perm;
  if (bid < 112) { swsrc = w1; sld = FF_ * 2; kb = bid / 14; nb = bid - kb * 14; sks = 32; sdst = WS_W1; perm = false; }
  else if (bid < 224) { int v = bid - 112; swsrc = w2; sld = D_ * 2; kb = v >> 2; nb = v & 3; sks = 112; sdst = WS_W2; perm = true; }
  else { int v = bid - 224; swsrc = wo; sld = D_ * 2; kb = v >> 2; nb = v & 3; sks = 32; sdst = WS_WO; perm = true; }
  const int h = lane >> 5, q = (lane & 15) >> 2;
  const unsigned cb = (16 * ((lane >> 4) & 1) + 4 * (lane & 3)) * 2;
  WAIT_LGKM0();
  BARRIER();
  {
    int islot = 2 * K1_SLOT;
#pragma nounroll
    for (int s = 2; s < 8; ++s) {
      wait_vm<7>(); flag_add(sm, K1_READY(s - 2), lane);
      if (s >= 5) flag_wait(sm, K1_DONE(s - 5), 8);
      issue(s, islot);
      islot += K1_SLOT;
      if (islot == K1_NSLOT * K1_SLOT) islot = 0;
    }
  }
  wait_vm<7>(); flag_add(sm, K1_READY(6), lane);
#pragma unroll
  for (int i = 0; i < 4; ++i) {
    const int p = 4 * lw + i;
    glds16(swsrc + (size_t)(kb * 64 + 4 * p) * sld + nb * 256, (unsigned)((lane >> 4) * sld + (lane & 15) * 16),
           sbase + K1_SIDE + p * 1024);
  }
  wait_vm<4>(); flag_add(sm, K1_READY(7), lane);
  WAIT_VM(0);
  {
    const int r0 = perm ? 4 * h : 8 * h, r1 = perm ? 8 + 4 * h : 8 * h + 4;
    const unsigned o0 = K1_SIDE + (16 * lw + r0 + q) * 256 + cb, o1 = K1_SIDE + (16 * lw + r1 + q) * 256 + cb;
#pragma unroll
    for (int nt = 0; nt < 4; ++nt) {
      bf16x8 f = cat8(lds_tr(sm, o0 + nt * 64), lds_tr(sm, o1 + nt * 64));
      st16(ws_rsrc(ws), (unsigned)sdst + ((nb * 4 + nt) * sks + kb * 4 + lw) * 1024 + lane * 16, f);
    }
  }
}

extern "C" __global__ __launch_bounds__(768) void k1_qkv(const char* __restrict__ x, const char* __restrict__ wqkv,
                                              const char* __restrict__ w1, const char* __restrict__ w2,
                                              const char* __restrict__ wo, char* __restrict__ ws) {
  extern __shared__ __attribute__((aligned(16))) char sm[];
  const int lane = threadIdx.x & 63, wave = __builtin_amdgcn_readfirstlane(threadIdx.x >> 6);
  const int bid = blockIdx.x, xcd = bid & 7, idx = bid >> 3;
  const int rg = 4 * (xcd >> 1) + (idx & 3), cg = 8 * (xcd & 1) + (idx >> 2);
  if (threadIdx.x >= 256 && threadIdx.x < 272) *(unsigned*)(sm + K1_FLG + (threadIdx.x - 256) * 4) = 0u;
  if (wave >= 4) { WAIT_LGKM0(); BARRIER(); }
  if (wave < 4) {
    const int rot = ((idx >> 2) + 2 * (idx & 3)) & 7;
    k1_loader_wave(sm, lds_addr(sm), x, wqkv, w1, w2, wo, wave, lane, bid, rg, cg, rot, ws);
  } else if (wave < 8) {
    if (cg < 10) k1_gemm_wave<0>(sm, wave - 4, lane, rg, cg, ws);
    else if (cg == 10) k1_gemm_wave<1>(sm, wave - 4, lane, rg, cg, ws);
    else k1_gemm_wave<3>(sm, wave - 4, lane, rg, cg, ws);
  } else {
    if (bid == 0) st16(ws_rsrc(ws), (unsigned)WS_FLAG + (wave - 8) * 1024 + lane * 16, u32x4{0u, 0u, 0u, 0u});
#pragma unroll
    for (int i = 0; i < 2; ++i)
      st16(ws_rsrc(ws), (unsigned)WS_OF + bid * 8192 + (wave - 8) * 2048 + i * 1024 + lane * 16, u32x4{~0u, ~0u, ~0u, ~0u});
    k1_stats_wave(sm, x, wave - 8, lane, rg, cg, ws);
  }
}

constexpr int K2_FLG = KVHEAD;
constexpr int K2_LDS = K2_FLG + 64;
constexpr float SM_DELTA = 60.f;
typedef __attribute__((address_space(1))) unsigned gu32;
#define X_AUXB 16
#define SB() __builtin_amdgcn_sched_barrier(0)

DI float xhalf_max(float v) {
  auto r = __builtin_amdgcn_permlane32_swap(__float_as_uint(v), __float_as_uint(v), false, false);
  return fmaxf(__uint_as_float(r[0]), __uint_as_float(r[1]));
}
DI float max16(const f32x16& s) {
  float m01 = fmaxf(fmaxf(s[0], s[1]), s[2]), m23 = fmaxf(fmaxf(s[3], s[4]), s[5]);
  float m45 = fmaxf(fmaxf(s[6], s[7]), s[8]), m67 = fmaxf(fmaxf(s[9], s[10]), s[11]);
  float m89 = fmaxf(fmaxf(s[12], s[13]), s[14]);
  return fmaxf(fmaxf(fmaxf(m01, m23), m45), fmaxf(fmaxf(m67, m89), s[15]));
}
DI void lds_signal(char* sm, int word, int lane) { flag_add(sm, K2_FLG + 4 * word, lane); }
DI void lds_wait(char* sm, int word, unsigned target) { flag_wait(sm, K2_FLG + 4 * word, target); }

template <int NKS, int PF, int AUXB>
DI void dgemm64_ring(__amdgpu_buffer_rsrc_t rs, unsigned a0, unsigned a1, unsigned b0, unsigned b1, int rot, f32x16 (&acc)[4]) {
  u32x4 A0[PF], A1[PF], B0[PF], B1[PF];
  int ko = rot * 1024;
#pragma unroll
  for (int kk = 0; kk < NKS + PF - 1; ++kk) {
    if (kk < NKS) {
      const int sl = kk % PF;
      A0[sl] = __builtin_amdgcn_raw_buffer_load_b128(rs, a0, ko, 0);
      B0[sl] = __builtin_amdgcn_raw_buffer_load_b128(rs, b0, ko, AUXB);
      A1[sl] = __builtin_amdgcn_raw_buffer_load_b128(rs, a1, ko, 0);
      B1[sl] = __builtin_amdgcn_raw_buffer_load_b128(rs, b1, ko, AUXB);
      ko += 1024;
      if (ko == NKS * 1024) ko = 0;
    }
    SB();
    if (kk >= PF - 1) {
      const int sl = (kk - (PF - 1)) % PF;
      const bf16x8 fa0 = __builtin_bit_cast(bf16x8, A0[sl]), fa1 = __builtin_bit_cast(bf16x8, A1[sl]);
      const bf16x8 fb0 = __builtin_bit_cast(bf16x8, B0[sl]), fb1 = __builtin_bit_cast(bf16x8, B1[sl]);
#define X_GAP
      acc[0] = MFMA(fa0, fb0, acc[0]);
      SB(); X_GAP; SB();
      acc[2] = MFMA(fa1, fb0, acc[2]);
      SB(); X_GAP; SB();
      acc[1] = MFMA(fa0, fb1, acc[1]);
      SB(); X_GAP; SB();
      acc[3] = MFMA(fa1, fb1, acc[3]);
    }
    SB();
  }
  asm volatile("" : "+v"(acc[0]), "+v"(acc[1]), "+v"(acc[2]), "+v"(acc[3]));
}

DI void k2_ffn_wave(char* sm, const char* __restrict__ x, char* __restrict__ ws, char* __restrict__ out, int w, int lane, int panel, int ci, int head) {
  {
    const unsigned sb = lds_addr(sm) + w * 1024, dl = lane * 16;
    const char* kvg = ws + WS_KV + (size_t)head * KVHEAD + w * 1024;
#pragma unroll
    for (int i = 0; i < 8; ++i) glds16(kvg + i * 4096, dl, sb + i * 4096);
    WAIT_LGKM0();
    BARRIER();
    WAIT_VM(4);
    lds_signal(sm, 0, lane);
#pragma unroll
    for (int i = 8; i < 16; ++i) glds16(kvg + i * 4096, dl, sb + i * 4096);
    WAIT_VM(4);
    lds_signal(sm, 1, lane);
#pragma unroll
    for (int i = 16; i < 34; ++i) glds16(kvg + i * 4096, dl, sb + i * 4096);
    WAIT_VM(0);
    lds_signal(sm, 2, lane);
  }
#define X_FPRIO 2
  __builtin_amdgcn_s_setprio(X_FPRIO);
  const __amdgpu_buffer_rsrc_t rs = __builtin_amdgcn_make_buffer_rsrc((void*)ws, 0, (int)WS_END, 0x00020000);
  gu32* flags = (gu32*)(ws + WS_FLAG) + panel * 128;
  STAMPT(ws, 2, 0);
  if (ci < 28) {
    const int rt0 = panel * 8 + 2 * w, nt0 = 2 * ci;
    const unsigned a0 = (unsigned)WS_W1 + nt0 * 32768 + lane * 16, b0 = (unsigned)WS_XN + rt0 * 32768 + lane * 16;
    f32x16 acc[4] = {};
#define X_ROT1 ci
    dgemm64_ring<32, 5, 0>(rs, a0, a0 + 32768, b0, b0 + 32768, X_ROT1, acc);
    STAMPT(ws, 2, 5);
#pragma unroll
    for (int i = 0; i < 2; ++i)
#pragma unroll
      for (int j = 0; j < 2; ++j) {
        f32x16 v = acc[2 * i + j];
#pragma unroll
        for (int e = 0; e < 16; ++e) v[e] = fmaxf(v[e], 0.f);
        const unsigned dst = (unsigned)WS_HF + ((rt0 + j) * 112 + (nt0 + i) * 2) * 1024 + lane * 16;
        __builtin_amdgcn_raw_buffer_store_b128(__builtin_bit_cast(u32x4, pack_lo(v)), rs, dst, 0, 16);
        __builtin_amdgcn_raw_buffer_store_b128(__builtin_bit_cast(u32x4, pack_hi(v)), rs, dst + 1024, 0, 16);
      }
    WAIT_VM(0);
    if (lane == 0) __hip_atomic_store(flags + w * 32 + ci, 1u, __ATOMIC_RELAXED, __HIP_MEMORY_SCOPE_AGENT);
  }
  STAMPT(ws, 2, 1);
  const int rp2 = ci & 3, np2 = ci >> 2;
  {
    __builtin_amdgcn_s_setprio(0);
    gu32* f = flags + rp2 * 32 + 7 * w + min(lane, 6);
    for (unsigned spins = 0; spins < (1u << 20); ++spins) {
      unsigned v = __hip_atomic_load(f, __ATOMIC_RELAXED, __HIP_MEMORY_SCOPE_AGENT);
      if (__all(v == 1u)) break;
      __builtin_amdgcn_s_sleep(8);
    }
    __builtin_amdgcn_fence(__ATOMIC_ACQUIRE, "wavefront");
    asm volatile("" ::: "memory");
  }
  STAMPT(ws, 2, 2);
  __builtin_amdgcn_s_setprio(X_FPRIO);
  const int rt0 = panel * 8 + 2 * rp2, nt0 = 2 * np2;
  f32x16 acc[4] = {};
  {
    const unsigned a0 = (unsigned)WS_W2 + (nt0 * 112 + 28 * w) * 1024 + lane * 16;
    const unsigned b0 = (unsigned)WS_HF + (rt0 * 112 + 28 * w) * 1024 + lane * 16;
    dgemm64_ring<28, 5, X_AUXB>(rs, a0, a0 + 112 * 1024, b0, b0 + 112 * 1024, (7 * rp2 + 3 * np2) % 28, acc);
  }
  STAMPT(ws, 2, 3);
  const int h = lane >> 5, r31 = lane & 31;
  const size_t eoff = ((size_t)((rt0 + (w & 1)) * 32 + r31) * D_ + (nt0 + (w >> 1)) * 32 + 4 * h) * 2;
  u32x2 xv[4];
#pragma unroll
  for (int g = 0; g < 4; ++g) xv[g] = *(const u32x2*)(x + eoff + 16 * g);
  __builtin_amdgcn_s_setprio(0);
  flag_wait<6>(sm, K2_FLG + 12, 8);
  {
    const int wl = 65536 + w * 16384;
    const char* wog = ws + WS_WO + (size_t)(nt0 * 32 + 8 * w) * 1024;
#pragma unroll
    for (int i = 0; i < 16; ++i) glds16(wog + (i >> 3) * 32768 + (i & 7) * 1024, lane * 16, lds_addr(sm) + wl + i * 1024);
    gu32* f = (gu32*)(ws + WS_OF + ((rt0 + ((lane >> 3) & 1)) * 32 + 8 * w + (lane & 7)) * 1024 + 1020);
    for (unsigned spins = 0; spins < (1u << 20); ++spins) {
      unsigned v = __hip_atomic_load(f, __ATOMIC_RELAXED, __HIP_MEMORY_SCOPE_AGENT);
      if (__all(v != ~0u)) break;
      __builtin_amdgcn_s_sleep(2);
    }
    asm volatile("" ::: "memory");
    STAMPT(ws, 2, 6);
    const unsigned b0 = (unsigned)WS_OF + (rt0 * 32 + 8 * w) * 1024 + lane * 16;
    u32x4 B[16];
    for (unsigned spins = 0; spins < (1u << 16); ++spins) {
      asm volatile("" ::: "memory");
#pragma unroll
      for (int i = 0; i < 16; ++i) B[i] = __builtin_amdgcn_raw_buffer_load_b128(rs, b0, (i & 1) * 32768 + (i >> 1) * 1024, 16);
      unsigned m = 0;
#pragma unroll
      for (int i = 0; i < 16; ++i) m = max(max(m, B[i][0]), max(B[i][1], max(B[i][2], B[i][3])));
      if (__builtin_expect(!__any(m == ~0u), 1)) break;
    }
    WAIT_VM(0);
#pragma unroll
    for (int ks = 0; ks < 8; ++ks) {
      const bf16x8 fa0 = lds_b128(sm, wl + ks * 1024 + lane * 16), fa1 = lds_b128(sm, wl + 8192 + ks * 1024 + lane * 16);
      const bf16x8 fb0 = __builtin_bit_cast(bf16x8, B[2 * ks]), fb1 = __builtin_bit_cast(bf16x8, B[2 * ks + 1]);
      acc[0] = MFMA(fa0, fb0, acc[0]);
      acc[2] = MFMA(fa1, fb0, acc[2]);
      acc[1] = MFMA(fa0, fb1, acc[1]);
      acc[3] = MFMA(fa1, fb1, acc[3]);
    }
  }
  {
    f32x4* red = (f32x4*)sm + lane;
#pragma unroll
    for (int t = 0; t < 4; ++t)
#pragma unroll
      for (int g = 0; g < 4; ++g)
        red[((t * 4 + w) * 4 + g) * 64] = f32x4{acc[t][4 * g], acc[t][4 * g + 1], acc[t][4 * g + 2], acc[t][4 * g + 3]};
    WAIT_LGKM0();
    lds_signal(sm, 4, lane);
    lds_wait(sm, 4, 4);
#pragma unroll
    for (int g = 0; g < 4; ++g) {
      f32x4 v = red[((w * 4 + 0) * 4 + g) * 64];
#pragma unroll
      for (int src = 1; src < 4; ++src) v += red[((w * 4 + src) * 4 + g) * 64];
      v[0] += bf_lo(xv[g][0]); v[1] += bf_hi(xv[g][0]); v[2] += bf_lo(xv[g][1]); v[3] += bf_hi(xv[g][1]);
      *(bf16x4*)(out + eoff + 16 * g) = __builtin_convertvector(v, bf16x4);
    }
  }
  STAMPT(ws, 2, 4);
}

extern "C" __global__ __launch_bounds__(768) void k2_attn(const char* __restrict__ x, char* __restrict__ ws, char* __restrict__ out) {
  extern __shared__ __attribute__((aligned(16))) char sm[];
  const int lane = threadIdx.x & 63, wave = __builtin_amdgcn_readfirstlane(threadIdx.x >> 6);
  const int bid = blockIdx.x, xcd = bid & 7, idx = bid >> 3;
  if (threadIdx.x < 16) *(unsigned*)(sm + K2_FLG + threadIdx.x * 4) = 0u;
  const int head = xcd * 4 + (idx >> 3), qb = idx & 7, rt = qb * 8 + wave;
  if (wave >= 8) {
    k2_ffn_wave(sm, x, ws, out, wave - 8, lane, xcd, idx, head);
    return;
  }
  STAMPT(ws, 2, 0);
  const bf16x8 qf = *((const bf16x8*)(ws + WS_QF) + ((size_t)(head * 64 + rt) * 64 + lane));
  const int h = lane >> 5, r31 = lane & 31;
  const unsigned koff = lane * 16;
  const unsigned voff = 1024 + ((r31 < 16) ? vslot(r31, h) : 15) * 16;
  WAIT_LGKM0();
  BARRIER();
  f32x16 negm = {}, O = {};
#define EXP(v, i) v[i] = __builtin_amdgcn_exp2f(v[i])
#define TILE_STEP(SC, SN, OFFK2, OFFV0N, OFFV1)                                   \
  {                                                                               \
    SN = MFMA(kn, qf, negm);                                                      \
    SB();                                                                         \
    kn = lds_b128(kp, OFFK2);                                                     \
    EXP(SC, 0); EXP(SC, 1); EXP(SC, 2); EXP(SC, 3); EXP(SC, 4); EXP(SC, 5);       \
    SB();                                                                         \
    O = MFMA(v1p, p1p, O);                                                        \
    SB();                                                                         \
    v1p = lds_b128(vp, OFFV1);                                                    \
    EXP(SC, 6); EXP(SC, 7);                                                       \
    bf16x8 p0 = pack_lo(SC);                                                      \
    SB();                                                                         \
    EXP(SC, 8); EXP(SC, 9);                                                       \
    SB();                                                                         \
    O = MFMA(v0, p0, O);                                                          \
    SB();                                                                         \
    v0 = lds_b128(vp, OFFV0N);                                                    \
    EXP(SC, 10); EXP(SC, 11); EXP(SC, 12); EXP(SC, 13); EXP(SC, 14); EXP(SC, 15); \
    p1p = pack_hi(SC);                                                            \
    SB();                                                                         \
  }
#define TILE4()                                                        \
  TILE_STEP(sA, sB, 2 * KVTILE, 1 * KVTILE, 0 * KVTILE + VBLK);        \
  TILE_STEP(sB, sA, 3 * KVTILE, 2 * KVTILE, 1 * KVTILE + VBLK);        \
  TILE_STEP(sA, sB, 4 * KVTILE, 3 * KVTILE, 2 * KVTILE + VBLK);        \
  TILE_STEP(sB, sA, 5 * KVTILE, 4 * KVTILE, 3 * KVTILE + VBLK);        \
  kp += 4 * KVTILE; vp += 4 * KVTILE;
  {
    const char* kp = sm + koff;
    const char* vp = sm + voff;
#define X_SW 20
    lds_wait(sm, 0, 4);
    STAMPT(ws, 2, 1);
    const bf16x8 k0 = lds_b128(kp, 0);
    bf16x8 kn = lds_b128(kp, KVTILE), v0 = lds_b128(vp, 0), v1p = lds_b128(vp, VBLK), p1p = {};
    {
      const f32x16 z = {};
      f32x16 s0 = MFMA(k0, qf, z), s1 = MFMA(kn, qf, z);
      const float nm = -(xhalf_max(fmaxf(max16(s0), max16(s1))) + SM_DELTA);
#pragma unroll
      for (int e = 0; e < 16; ++e) negm[e] = nm;
    }
    f32x16 sA = MFMA(k0, qf, negm), sB;
    int ev = 4;
    for (int t = 0; t < 64; t += 4) {
      if (__builtin_expect(t == ev, 0)) {
        lds_wait(sm, t == 4 ? 1 : 2, 4);
        if (t != 4 && wave >= 4) __builtin_amdgcn_s_setprio(1);
        ev = (t == 4) ? X_SW : -100;
      }
      TILE4();
      asm volatile("" : "+v"(O), "+v"(sA), "+v"(p1p));
    }
    O = MFMA(v1p, p1p, O);
  }
  STAMPT(ws, 2, 2);
  float l = O[8];
  bool ok = (l < 0x1p100f);
#pragma unroll
  for (int e = 0; e < 8; ++e) ok = ok && (fabsf(O[e]) < 0x1p126f);
  if (!__all(ok)) {
    float m = -INFINITY;
#pragma unroll
    for (int e = 0; e < 16; ++e) O[e] = 0.f;
    for (int t = 0; t < 64; ++t) {
      f32x16 z = {};
      f32x16 sc = MFMA(lds_b128(sm, koff + t * KVTILE), qf, z);
      bf16x8 v0 = lds_b128(sm, voff + t * KVTILE), v1 = lds_b128(sm, voff + t * KVTILE + VBLK);
      float mn = fmaxf(m, xhalf_max(max16(sc)));
      float alpha = __builtin_amdgcn_exp2f(m - mn);
      m = mn;
#pragma unroll
      for (int e = 0; e < 16; ++e) { O[e] *= alpha; sc[e] = __builtin_amdgcn_exp2f(sc[e] - mn); }
      O = MFMA(v0, pack_lo(sc), O);
      O = MFMA(v1, pack_hi(sc), O);
    }
    l = O[8];
  }
  const float rl = 1.0f / l;
#pragma unroll
  for (int e = 0; e < 8; ++e) O[e] *= rl;
  u32x4 po = __builtin_bit_cast(u32x4, pack_lo(O));
#pragma unroll
  for (int e = 0; e < 4; ++e) po[e] = min(po[e], 0xFFFFFFFEu);
  __builtin_amdgcn_raw_buffer_store_b128(po, ws_rsrc(ws), (unsigned)WS_OF + (rt * 32 + head) * 1024 + lane * 16, 0, 16);
  WAIT_LGKM0();
  lds_signal(sm, 3, lane);
  STAMPT(ws, 2, 3);
}

extern "C" __global__ void k9_end() {}

extern "C" void kernel_launch(void* const* d_in, const int* in_sizes, int n_in, void* d_out, int out_size, void* d_ws,
                              size_t ws_size, hipStream_t stream) {
  const char* x = (const char*)d_in[0];
  const char* wqkv = (const char*)d_in[1];
  const char* wo = (const char*)d_in[2];
  const char* w1 = (const char*)d_in[4];
  const char* w2 = (const char*)d_in[5];
  char* ws = (char*)d_ws;
  (void)hipFuncSetAttribute((const void*)k1_qkv, hipFuncAttributeMaxDynamicSharedMemorySize, K1_LDS);
  (void)hipFuncSetAttribute((const void*)k2_attn, hipFuncAttributeMaxDynamicSharedMemorySize, K2_LDS);
  hipLaunchKernelGGL(k1_qkv, dim3(256), dim3(768), K1_LDS, stream, x, wqkv, w1, w2, wo, ws);
  hipLaunchKernelGGL(k2_attn, dim3(256), dim3(768), K2_LDS, stream, x, ws, (char*)d_out);
}
```

```cpp
#include <hip/hip_runtime.h>
#include <hip/hip_bf16.h>
#include <stdint.h>

typedef __attribute__((ext_vector_type(8))) __bf16 bf16x8;
typedef __attribute__((ext_vector_type(4))) __bf16 bf16x4;
typedef __attribute__((ext_vector_type(16))) float f32x16;
typedef __attribute__((ext_vector_type(8))) float f32x8;
typedef __attribute__((ext_vector_type(4))) float f32x4;
typedef __attribute__((ext_vector_type(2))) float f32x2;
typedef __attribute__((ext_vector_type(4))) unsigned u32x4;
typedef __attribute__((ext_vector_type(2))) unsigned u32x2;
#define LDS_AS __attribute__((address_space(3)))
#define DI __device__ __forceinline__
#define MFMA(a, b, c) __builtin_amdgcn_mfma_f32_32x32x16_bf16((a), (b), (c), 0, 0, 0)

constexpr int S_ = 2048, D_ = 512, FF_ = 1792, NH_ = 32;
constexpr int KVTILE = 2112;
constexpr int VBLK = 544;
constexpr int KVHEAD = 136 * 1024;
constexpr size_t WS_QF = 0;
constexpr size_t WS_KV = WS_QF + (size_t)2 * 1024 * 1024;
constexpr size_t WS_XN = WS_KV + (size_t)NH_ * KVHEAD;
constexpr size_t WS_W1 = WS_XN + (size_t)2 * 1024 * 1024;
constexpr size_t WS_W2 = WS_W1 + (size_t)1792 * 1024;
constexpr size_t WS_WO = WS_W2 + (size_t)1792 * 1024;
constexpr size_t WS_HF = WS_WO + (size_t)512 * 1024;
constexpr size_t WS_OF = WS_HF + (size_t)64 * 112 * 1024;
constexpr size_t WS_Y = WS_OF + (size_t)2 * 1024 * 1024;
constexpr size_t WS_FLAG = WS_Y + (size_t)4 * 1024 * 1024;
constexpr size_t WS_FLAG2 = WS_FLAG + 4096;
constexpr size_t WS_END = WS_FLAG2 + 8192;

#define STAMPT(ws, kid, i)
constexpr float QSCALE = 0.25f * 1.4426950408889634f;

DI unsigned lds_addr(const void* p) { return (unsigned)(uintptr_t)(LDS_AS const char*)p; }
DI void glds16(const void* sbase, unsigned voff, unsigned ldst) {
  unsigned keep;
  asm volatile("s_mov_b32 %0, m0\n\ts_mov_b32 m0, %3\n\ts_nop 0\n\tglobal_load_lds_dwordx4 %1, %2\n\ts_mov_b32 m0, %0"
               : "=&s"(keep) : "v"(voff), "s"(sbase), "s"(ldst) : "memory");
}
#define WAIT_VM(n) asm volatile("s_waitcnt vmcnt(" #n ")" ::: "memory")
#define WAIT_LGKM0() asm volatile("s_waitcnt lgkmcnt(0)" ::: "memory")
#define BARRIER() __builtin_amdgcn_s_barrier()
#define TBAR(acc) __builtin_amdgcn_s_barrier()
#define TVM(acc, stmt) stmt
#define TBAR_OUT(ws, acc, i)
template <int N> DI void wait_vm() { asm volatile("s_waitcnt vmcnt(%0)" ::"i"(N) : "memory"); }

#define X_STAUX 16
DI __amdgpu_buffer_rsrc_t ws_rsrc(const void* ws) { return __builtin_amdgcn_make_buffer_rsrc((void*)ws, 0, (int)WS_END, 0x00020000); }
template <typename T> DI void st16(__amdgpu_buffer_rsrc_t rs, unsigned off, T v) {
  __builtin_amdgcn_raw_buffer_store_b128(__builtin_bit_cast(u32x4, v), rs, off, 0, X_STAUX);
}
DI bf16x8 lds_b128(const char* sm, unsigned off) { return *(const bf16x8*)(sm + off); }
DI bf16x4 lds_tr(const char* sm, unsigned off) {
  return __builtin_amdgcn_ds_read_tr16_b64_v4bf16((LDS_AS bf16x4*)(LDS_AS char*)(sm + off));
}
DI bf16x8 cat8(bf16x4 lo, bf16x4 hi) { return __builtin_shufflevector(lo, hi, 0, 1, 2, 3, 4, 5, 6, 7); }
DI bf16x8 pack_lo(const f32x16& v) { return __builtin_convertvector(__builtin_shufflevector(v, v, 0, 1, 2, 3, 4, 5, 6, 7), bf16x8); }
DI bf16x8 pack_hi(const f32x16& v) { return __builtin_convertvector(__builtin_shufflevector(v, v, 8, 9, 10, 11, 12, 13, 14, 15), bf16x8); }
DI float bf_lo(unsigned u) { return __uint_as_float(u << 16); }
DI float bf_hi(unsigned u) { return __uint_as_float(u & 0xffff0000u); }
DI int vslot(int d, int h) {
  int s = (d < 4) ? d : (d >= 12) ? d - 8 : (d < 11) ? d + 4 : -1;
  return (s >= 0) ? (16 * h + s) : (32 + h);
}

DI LDS_AS unsigned* lds_word(char* sm, int off) { return (LDS_AS unsigned*)(LDS_AS char*)(sm + off); }
DI void flag_add(char* sm, int off, int lane) {
  if (lane == 0) __hip_atomic_fetch_add(lds_word(sm, off), 1u, __ATOMIC_RELAXED, __HIP_MEMORY_SCOPE_WORKGROUP);
  asm volatile("" ::: "memory");
}
DI unsigned flag_peek(char* sm, int off) { return __hip_atomic_load(lds_word(sm, off), __ATOMIC_RELAXED, __HIP_MEMORY_SCOPE_WORKGROUP); }
template <int SLEEP = 1>
DI void flag_wait(char* sm, int off, unsigned target) {
  while ((unsigned)__builtin_amdgcn_readfirstlane(flag_peek(sm, off)) < target) __builtin_amdgcn_s_sleep(SLEEP);
  asm volatile("" ::: "memory");
}

constexpr int K1_SLOT = 28672, K1_WOFF = 16384, K1_NSLOT = 5;
constexpr int K1_STATS = K1_SLOT * K1_NSLOT;
constexpr int K1_WPART = K1_STATS + 1024;
constexpr int K1_FLG = K1_WPART + 768;
constexpr int K1_SIDE = K1_FLG + 256;
constexpr int K1_LDS = K1_SIDE + 16384;
#define K1_READY(s) (K1_FLG + 4 * (s))
#define K1_DONE(s) (K1_FLG + 32 + 4 * (s))
#define K1_FIN (K1_FLG + 44)

template <int NV>
DI void k1_gemm_wave(char* sm, int w, int lane, int rg, int cg, char* ws) {
  const int h = lane >> 5, r31 = lane & 31;
  STAMPT(ws, 1, 0);
  unsigned aoff[4];
  {
    const int row = 32 * w + r31;
#pragma unroll
    for (int ks = 0; ks < 4; ++ks) aoff[ks] = row * 128 + (((2 * ks + h) ^ ((row >> 1) & 7)) << 4);
  }
  const unsigned boff = K1_WOFF + (8 * h + ((lane & 15) >> 2)) * 192 + (16 * ((lane >> 4) & 1) + 4 * (lane & 3)) * 2;
  f32x16 acc[3] = {};
  bf16x8 fa[4], fb[4][3];
#pragma unroll
  for (int i = 2; i < 4; ++i) {
    fa[i] = bf16x8{};
#pragma unroll
    for (int j = 0; j < 3; ++j) fb[i][j] = bf16x8{};
  }
#define K1_MMA(ps)                                                        \
  _Pragma("unroll") for (int j = 0; j < 3; ++j) {                         \
    if (j >= 3 - NV) acc[j] = MFMA(fa[ps], fb[ps][j], acc[j]);   \
    else acc[j] = MFMA(fb[ps][j], fa[ps], acc[j]);               \
  }
  int slot = 0;
  unsigned rdy = 0;
#pragma nounroll
  for (int s = 0; s < 8; ++s) {
    if ((unsigned)__builtin_amdgcn_readfirstlane(rdy) < 4u) flag_wait(sm, K1_READY(s), 4);
    asm volatile("" ::: "memory");
    if (s == 0) STAMPT(ws, 1, 1);
    if (s == 4) STAMPT(ws, 1, 3);
    const char* sl = sm + slot;
#pragma unroll
    for (int ks = 0; ks < 4; ++ks) {
      if (ks == 2) rdy = flag_peek(sm, K1_READY(s + 1));
      fa[ks] = lds_b128(sl, aoff[ks]);
#pragma unroll
      for (int j = 0; j < 3; ++j)
        fb[ks][j] = cat8(lds_tr(sl, boff + ks * 16 * 192 + 64 * j), lds_tr(sl, boff + (ks * 16 + 4) * 192 + 64 * j));
      __builtin_amdgcn_sched_barrier(0);
      K1_MMA((ks + 2) & 3);
      __builtin_amdgcn_sched_barrier(0);
      if (ks == 1 && s >= 1 && s <= 3) flag_add(sm, K1_DONE(s - 1), lane);
    }
    slot += K1_SLOT;
    if (slot == K1_NSLOT * K1_SLOT) slot = 0;
  }
  K1_MMA(2);
  K1_MMA(3);
  asm volatile("" : "+v"(acc[0]), "+v"(acc[1]), "+v"(acc[2]));
  STAMPT(ws, 1, 5);
  flag_wait(sm, K1_FIN, 4);
  STAMPT(ws, 1, 6);
  const int rt = rg * 4 + w;
  const __amdgpu_buffer_rsrc_t rs = ws_rsrc(ws);
  const f32x2* stats = (const f32x2*)(sm + K1_STATS) + 32 * w;
  const float* wsum = (const float*)(sm + K1_WPART);
#pragma unroll
  for (int j = 0; j < 3; ++j) {
    const int ct = cg * 3 + j, role = ct >> 4, hp = (ct & 15) * 2;
    f32x16 v;
    if (j >= 3 - NV) {
      const float wsn = wsum[32 * j + r31] + wsum[96 + 32 * j + r31];
#pragma unroll
      for (int g = 0; g < 4; ++g) {
        f32x4 s0 = *(const f32x4*)(stats + 8 * g + 4 * h), s1 = *(const f32x4*)(stats + 8 * g + 4 * h + 2);
        v[4 * g + 0] = acc[j][4 * g + 0] * s0[0] + s0[1] * wsn;
        v[4 * g + 1] = acc[j][4 * g + 1] * s0[2] + s0[3] * wsn;
        v[4 * g + 2] = acc[j][4 * g + 2] * s1[0] + s1[1] * wsn;
        v[4 * g + 3] = acc[j][4 * g + 3] * s1[2] + s1[3] * wsn;
      }
      const unsigned base = (unsigned)WS_KV + (hp + (r31 >> 4)) * KVHEAD + rt * KVTILE + 1024;
      const int sl = vslot(r31 & 15, h);
      st16(rs, base + sl * 16, pack_lo(v));
      st16(rs, base + VBLK + sl * 16, pack_hi(v));
      if ((lane & 15) == 0)
        st16(rs, base + h * VBLK + 15 * 16, u32x4{0x3f803f80u, 0x3f803f80u, 0x3f803f80u, 0x3f803f80u});
    } else {
      f32x2 st = stats[r31];
      if (role == 0) { st[0] *= QSCALE; st[1] *= QSCALE; }
#pragma unroll
      for (int g = 0; g < 4; ++g) {
        f32x4 wsv = *(const f32x4*)(wsum + 32 * j + 8 * g + 4 * h) + *(const f32x4*)(wsum + 96 + 32 * j + 8 * g + 4 * h);
#pragma unroll
        for (int e = 0; e < 4; ++e) v[4 * g + e] = acc[j][4 * g + e] * st[0] + st[1] * wsv[e];
      }
      if (role == 0) {
        const unsigned base = (unsigned)WS_QF + (hp * 64 + rt) * 1024 + lane * 16;
        st16(rs, base, pack_lo(v));
        st16(rs, base + 64 * 1024, pack_hi(v));
      } else {
        const unsigned base = (unsigned)WS_KV + hp * KVHEAD + rt * KVTILE + lane * 16;
        st16(rs, base, pack_lo(v));
        st16(rs, base + KVHEAD, pack_hi(v));
      }
    }
  }
  STAMPT(ws, 1, 7);
}

#define MFMA16(a, b, c) __builtin_amdgcn_mfma_f32_16x16x32_bf16((a), (b), (c), 0, 0, 0)
DI void k1_stats_wave(char* sm, const char* __restrict__ xg, int hw, int lane, int rg, int cg, char* ws) {
  STAMPT(ws, 1, 0);
  const int r15 = lane & 15, kq = lane >> 4;
  unsigned aoff[4];
#pragma unroll
  for (int i = 0; i < 4; ++i) {
    const int row = 32 * hw + 16 * (i >> 1) + r15, c = 4 * (i & 1) + kq;
    aoff[i] = row * 128 + ((c ^ ((row >> 1) & 7)) << 4);
  }
  const unsigned boff = K1_WOFF + (32 * (hw & 1) + 8 * kq + (r15 >> 2)) * 192 + (48 * (hw >> 1) + 4 * (lane & 3)) * 2;
  const bf16x8 ones = __builtin_bit_cast(bf16x8, u32x4{0x3f803f80u, 0x3f803f80u, 0x3f803f80u, 0x3f803f80u});
  f32x4 gram[2] = {}, rsum[2] = {}, wacc[3] = {};
  bf16x8 xa[2][4] = {}, wb[2][3] = {};
  int slot = 0;
  u32x4 xr2[2];
#pragma unroll
  for (int i = 0; i < 2; ++i)
    xr2[i] = *(const u32x4*)(xg + (size_t)(rg * 128 + cg * 8 + (lane & 7)) * 1024 + ((lane >> 3) + 8 * (2 * hw + i)) * 16);
  auto math = [&](const bf16x8 (&xp)[4], const bf16x8 (&wp)[3]) {
#pragma unroll
    for (int i = 0; i < 4; ++i) {
      gram[i >> 1] = MFMA16(xp[i], xp[i], gram[i >> 1]);
      rsum[i >> 1] = MFMA16(xp[i], ones, rsum[i >> 1]);
    }
#pragma unroll
    for (int j = 0; j < 3; ++j) wacc[j] = MFMA16(ones, wp[j], wacc[j]);
  };
#pragma nounroll
  for (int s2 = 0; s2 < 4; ++s2) {
#pragma unroll
    for (int par = 0; par < 2; ++par) {
      flag_wait(sm, K1_READY(2 * s2 + par), 4);
      const char* sl = sm + slot;
#pragma unroll
      for (int i = 0; i < 4; ++i) xa[par][i] = lds_b128(sl, aoff[i]);
#pragma unroll
      for (int j = 0; j < 3; ++j) wb[par][j] = cat8(lds_tr(sl, boff + 32 * j), lds_tr(sl, boff + 4 * 192 + 32 * j));
      __builtin_amdgcn_sched_barrier(0);
      math(xa[par ^ 1], wb[par ^ 1]);
      __builtin_amdgcn_sched_barrier(0);
      WAIT_LGKM0();
      if (2 * s2 + par <= 2) flag_add(sm, K1_DONE(2 * s2 + par), lane);
      slot += K1_SLOT;
      if (slot == K1_NSLOT * K1_SLOT) slot = 0;
    }
  }
  math(xa[1], wb[1]);
  STAMPT(ws, 1, 5);
  if (lane < 16) {
#pragma unroll
    for (int j = 0; j < 3; ++j) *(float*)(sm + K1_WPART + ((hw & 1) * 96 + 48 * (hw >> 1) + 16 * j + lane) * 4) = wacc[j][0];
  }
  if ((r15 >> 2) == kq) {
#pragma unroll
    for (int g2 = 0; g2 < 2; ++g2) {
      const int e = r15 & 3;
      const float sq = e == 0 ? gram[g2][0] : e == 1 ? gram[g2][1] : e == 2 ? gram[g2][2] : gram[g2][3];
      const float sum = e == 0 ? rsum[g2][0] : e == 1 ? rsum[g2][1] : e == 2 ? rsum[g2][2] : rsum[g2][3];
      const int row = 32 * hw + 16 * g2 + r15;
      const float mean = sum * (1.f / 512.f);
      float var = sq * (1.f / 512.f) - mean * mean;
      if (__builtin_expect(!(var > 1e-3f * mean * mean), 0)) {
        const unsigned* xr = (const unsigned*)(xg + (size_t)(rg * 128 + row) * 1024);
        float acc = 0.f;
        for (int i = 0; i < 256; ++i) {
          const unsigned u = xr[i];
          const float a = bf_lo(u) - mean, b = bf_hi(u) - mean;
          acc = fmaf(a, a, fmaf(b, b, acc));
        }
        var = acc * (1.f / 512.f);
      }
      const float rstd = __builtin_amdgcn_rsqf(var + 1e-5f);
      *(f32x2*)(sm + K1_STATS + row * 8) = f32x2{rstd, -mean * rstd};
    }
  }
  WAIT_LGKM0();
  flag_add(sm, K1_FIN, lane);
  flag_wait(sm, K1_FIN, 4);
  STAMPT(ws, 1, 6);
  {
    const int p = lane & 7, lrow = cg * 8 + p;
    const f32x2 st = *(const f32x2*)(sm + K1_STATS + lrow * 8);
    const int grow = rg * 128 + lrow;
#pragma unroll
    for (int i = 0; i < 2; ++i) {
      const int c = (lane >> 3) + 8 * (2 * hw + i);
      const u32x4 u = xr2[i];
      f32x8 v;
#pragma unroll
      for (int e = 0; e < 4; ++e) { v[2 * e] = bf_lo(u[e]) * st[0] + st[1]; v[2 * e + 1] = bf_hi(u[e]) * st[0] + st[1]; }
      st16(ws_rsrc(ws), (unsigned)WS_XN + (((grow >> 5) * 32 + (c >> 1)) * 64 + (c & 1) * 32 + (grow & 31)) * 16,
           __builtin_convertvector(v, bf16x8));
    }
  }
  STAMPT(ws, 1, 7);
}

DI void k1_loader_wave(char* sm, unsigned sbase, const char* xg, const char* wqkv, const char* w1, const char* w2,
                       const char* wo, int lw, int lane, int bid, int rg, int cg, int rot, char* ws) {
  unsigned xoff[4], woff[3];
#pragma unroll
  for (int p = 0; p < 4; ++p) {
    int c = (lane & 7) ^ ((4 * p + (lane >> 4)) & 7);
    xoff[p] = (unsigned)((rg * 128 + 32 * lw + 8 * p + (lane >> 3)) * 1024 + c * 16);
  }
#pragma unroll
  for (int i = 0; i < 3; ++i) {
    int idx = 64 * (3 * lw + i) + lane;
    int row = idx / 12, ch = idx - row * 12;
    woff[i] = (unsigned)(row * 3072 + cg * 192 + ch * 16);
  }
  auto issue = [&](int s, int slot) {
    const int ks = (s + rot) & 7;
    const unsigned dst = sbase + slot;
#pragma unroll
    for (int i = 0; i < 4; ++i) glds16(xg + ks * 128, xoff[i], dst + lw * 4096 + i * 1024);
#pragma unroll
    for (int i = 0; i < 3; ++i) glds16(wqkv + (size_t)ks * 64 * 3072, woff[i], dst + K1_WOFF + lw * 3072 + i * 1024);
  };
  issue(0, 0);
  issue(1, K1_SLOT);
  const char* swsrc; int sld, kb, nb, sks; size_t sdst; bool perm;
  if (bid < 112) { swsrc = w1; sld = FF_ * 2; kb = bid / 14; nb = bid - kb * 14; sks = 32; sdst = WS_W1; perm = false; }
  else if (bid < 224) { int v = bid - 112; swsrc = w2; sld = D_ * 2; kb = v >> 2; nb = v & 3; sks = 112; sdst = WS_W2; perm = true; }
  else { int v = bid - 224; swsrc = wo; sld = D_ * 2; kb = v >> 2; nb = v & 3; sks = 32; sdst = WS_WO; perm = true; }
  const int h = lane >> 5, q = (lane & 15) >> 2;
  const unsigned cb = (16 * ((lane >> 4) & 1) + 4 * (lane & 3)) * 2;
  {
    int islot = 2 * K1_SLOT;
#pragma nounroll
    for (int s = 2; s < 8; ++s) {
      wait_vm<7>(); flag_add(sm, K1_READY(s - 2), lane);
      if (s >= 5) flag_wait(sm, K1_DONE(s - 5), 8);
      issue(s, islot);
      islot += K1_SLOT;
      if (islot == K1_NSLOT * K1_SLOT) islot = 0;
    }
  }
  wait_vm<7>(); flag_add(sm, K1_READY(6), lane);
#pragma unroll
  for (int i = 0; i < 4; ++i) {
    const int p = 4 * lw + i;
    glds16(swsrc + (size_t)(kb * 64 + 4 * p) * sld + nb * 256, (unsigned)((lane >> 4) * sld + (lane & 15) * 16),
           sbase + K1_SIDE + p * 1024);
  }
  wait_vm<4>(); flag_add(sm, K1_READY(7), lane);
  WAIT_VM(0);
  {
    const int r0 = perm ? 4 * h : 8 * h, r1 = perm ? 8 + 4 * h : 8 * h + 4;
    const unsigned o0 = K1_SIDE + (16 * lw + r0 + q) * 256 + cb, o1 = K1_SIDE + (16 * lw + r1 + q) * 256 + cb;
#pragma unroll
    for (int nt = 0; nt < 4; ++nt) {
      bf16x8 f = cat8(lds_tr(sm, o0 + nt * 64), lds_tr(sm, o1 + nt * 64));
      st16(ws_rsrc(ws), (unsigned)sdst + ((nb * 4 + nt) * sks + kb * 4 + lw) * 1024 + lane * 16, f);
    }
  }
}

extern "C" __global__ __launch_bounds__(768) void k1_qkv(const char* __restrict__ x, const char* __restrict__ wqkv,
                                              const char* __restrict__ w1, const char* __restrict__ w2,
                                              const char* __restrict__ wo, char* __restrict__ ws) {
  extern __shared__ __attribute__((aligned(16))) char sm[];
  const int lane = threadIdx.x & 63, wave = __builtin_amdgcn_readfirstlane(threadIdx.x >> 6);
  const int bid = blockIdx.x, xcd = bid & 7, idx = bid >> 3;
  const int rg = 4 * (xcd >> 1) + (idx & 3), cg = 8 * (xcd & 1) + (idx >> 2);
  if (threadIdx.x >= 256 && threadIdx.x < 272) *(unsigned*)(sm + K1_FLG + (threadIdx.x - 256) * 4) = 0u;
  WAIT_LGKM0();
  BARRIER();
  if (wave < 4) {
    const int rot = ((idx >> 2) + 2 * (idx & 3)) & 7;
    k1_loader_wave(sm, lds_addr(sm), x, wqkv, w1, w2, wo, wave, lane, bid, rg, cg, rot, ws);
  } else if (wave < 8) {
    if (cg < 10) k1_gemm_wave<0>(sm, wave - 4, lane, rg, cg, ws);
    else if (cg == 10) k1_gemm_wave<1>(sm, wave - 4, lane, rg, cg, ws);
    else k1_gemm_wave<3>(sm, wave - 4, lane, rg, cg, ws);
  } else {
    if (bid == 0) st16(ws_rsrc(ws), (unsigned)WS_FLAG + (wave - 8) * 1024 + lane * 16, u32x4{0u, 0u, 0u, 0u});
#pragma unroll
    for (int i = 0; i < 2; ++i)
      st16(ws_rsrc(ws), (unsigned)WS_OF + bid * 8192 + (wave - 8) * 2048 + i * 1024 + lane * 16, u32x4{~0u, ~0u, ~0u, ~0u});
    k1_stats_wave(sm, x, wave - 8, lane, rg, cg, ws);
  }
}

constexpr int K2_FLG = KVHEAD;
constexpr int K2_LDS = K2_FLG + 64;
constexpr float SM_DELTA = 80.f;
typedef __attribute__((address_space(1))) unsigned gu32;
#define X_AUXB 16
#define SB() __builtin_amdgcn_sched_barrier(0)

DI float xhalf_max(float v) {
  auto r = __builtin_amdgcn_permlane32_swap(__float_as_uint(v), __float_as_uint(v), false, false);
  return fmaxf(__uint_as_float(r[0]), __uint_as_float(r[1]));
}
DI float max16(const f32x16& s) {
  float m01 = fmaxf(fmaxf(s[0], s[1]), s[2]), m23 = fmaxf(fmaxf(s[3], s[4]), s[5]);
  float m45 = fmaxf(fmaxf(s[6], s[7]), s[8]), m67 = fmaxf(fmaxf(s[9], s[10]), s[11]);
  float m89 = fmaxf(fmaxf(s[12], s[13]), s[14]);
  return fmaxf(fmaxf(fmaxf(m01, m23), m45), fmaxf(fmaxf(m67, m89), s[15]));
}
DI void lds_signal(char* sm, int word, int lane) { flag_add(sm, K2_FLG + 4 * word, lane); }
DI void lds_wait(char* sm, int word, unsigned target) { flag_wait(sm, K2_FLG + 4 * word, target); }

template <int NKS, int PF, int AUXB>
DI void dgemm64_ring(__amdgpu_buffer_rsrc_t rs, unsigned a0, unsigned a1, unsigned b0, unsigned b1, int rot, f32x16 (&acc)[4]) {
  u32x4 A0[PF], A1[PF], B0[PF], B1[PF];
  int ko = rot * 1024;
#pragma unroll
  for (int kk = 0; kk < NKS + PF - 1; ++kk) {
    if (kk < NKS) {
      const int sl = kk % PF;
      A0[sl] = __builtin_amdgcn_raw_buffer_load_b128(rs, a0, ko, 0);
      B0[sl] = __builtin_amdgcn_raw_buffer_load_b128(rs, b0, ko, AUXB);
      A1[sl] = __builtin_amdgcn_raw_buffer_load_b128(rs, a1, ko, 0);
      B1[sl] = __builtin_amdgcn_raw_buffer_load_b128(rs, b1, ko, AUXB);
      ko += 1024;
      if (ko == NKS * 1024) ko = 0;
    }
    SB();
    if (kk >= PF - 1) {
      const int sl = (kk - (PF - 1)) % PF;
      const bf16x8 fa0 = __builtin_bit_cast(bf16x8, A0[sl]), fa1 = __builtin_bit_cast(bf16x8, A1[sl]);
      const bf16x8 fb0 = __builtin_bit_cast(bf16x8, B0[sl]), fb1 = __builtin_bit_cast(bf16x8, B1[sl]);
#define X_GAP
      acc[0] = MFMA(fa0, fb0, acc[0]);
      SB(); X_GAP; SB();
      acc[2] = MFMA(fa1, fb0, acc[2]);
      SB(); X_GAP; SB();
      acc[1] = MFMA(fa0, fb1, acc[1]);
      SB(); X_GAP; SB();
      acc[3] = MFMA(fa1, fb1, acc[3]);
    }
    SB();
  }
  asm volatile("" : "+v"(acc[0]), "+v"(acc[1]), "+v"(acc[2]), "+v"(acc[3]));
}

DI void k2_ffn_wave(char* sm, const char* __restrict__ x, char* __restrict__ ws, char* __restrict__ out, int w, int lane, int panel, int ci, int head) {
  {
    const unsigned sb = lds_addr(sm) + w * 1024, dl = lane * 16;
    const char* kvg = ws + WS_KV + (size_t)head * KVHEAD + w * 1024;
#pragma unroll
    for (int i = 0; i < 8; ++i) glds16(kvg + i * 4096, dl, sb + i * 4096);
    WAIT_LGKM0();
    BARRIER();
    WAIT_VM(4);
    lds_signal(sm, 0, lane);
#pragma unroll
    for (int i = 8; i < 16; ++i) glds16(kvg + i * 4096, dl, sb + i * 4096);
    WAIT_VM(4);
    lds_signal(sm, 1, lane);
#pragma unroll
    for (int i = 16; i < 34; ++i) glds16(kvg + i * 4096, dl, sb + i * 4096);
    WAIT_VM(0);
    lds_signal(sm, 2, lane);
  }
#define X_FPRIO 2
  __builtin_amdgcn_s_setprio(X_FPRIO);
  const __amdgpu_buffer_rsrc_t rs = __builtin_amdgcn_make_buffer_rsrc((void*)ws, 0, (int)WS_END, 0x00020000);
  gu32* flags = (gu32*)(ws + WS_FLAG) + panel * 128;
  STAMPT(ws, 2, 0);
  if (ci < 28) {
    const int rt0 = panel * 8 + 2 * w, nt0 = 2 * ci;
    const unsigned a0 = (unsigned)WS_W1 + nt0 * 32768 + lane * 16, b0 = (unsigned)WS_XN + rt0 * 32768 + lane * 16;
    f32x16 acc[4] = {};
#define X_ROT1 ci
    dgemm64_ring<32, 5, 0>(rs, a0, a0 + 32768, b0, b0 + 32768, X_ROT1, acc);
    STAMPT(ws, 2, 5);
#pragma unroll
    for (int i = 0; i < 2; ++i)
#pragma unroll
      for (int j = 0; j < 2; ++j) {
        f32x16 v = acc[2 * i + j];
#pragma unroll
        for (int e = 0; e < 16; ++e) v[e] = fmaxf(v[e], 0.f);
        const unsigned dst = (unsigned)WS_HF + ((rt0 + j) * 112 + (nt0 + i) * 2) * 1024 + lane * 16;
        __builtin_amdgcn_raw_buffer_store_b128(__builtin_bit_cast(u32x4, pack_lo(v)), rs, dst, 0, 16);
        __builtin_amdgcn_raw_buffer_store_b128(__builtin_bit_cast(u32x4, pack_hi(v)), rs, dst + 1024, 0, 16);
      }
    WAIT_VM(0);
    if (lane == 0) __hip_atomic_store(flags + w * 32 + ci, 1u, __ATOMIC_RELAXED, __HIP_MEMORY_SCOPE_AGENT);
  }
  STAMPT(ws, 2, 1);
  const int rp2 = ci & 3, np2 = ci >> 2;
  {
    __builtin_amdgcn_s_setprio(0);
    gu32* f = flags + rp2 * 32 + 7 * w + min(lane, 6);
    for (unsigned spins = 0; spins < (1u << 20); ++spins) {
      unsigned v = __hip_atomic_load(f, __ATOMIC_RELAXED, __HIP_MEMORY_SCOPE_AGENT);
      if (__all(v == 1u)) break;
      __builtin_amdgcn_s_sleep(8);
    }
    __builtin_amdgcn_fence(__ATOMIC_ACQUIRE, "wavefront");
    asm volatile("" ::: "memory");
  }
  STAMPT(ws, 2, 2);
  __builtin_amdgcn_s_setprio(X_FPRIO);
  const int rt0 = panel * 8 + 2 * rp2, nt0 = 2 * np2;
  f32x16 acc[4] = {};
  {
    const unsigned a0 = (unsigned)WS_W2 + (nt0 * 112 + 28 * w) * 1024 + lane * 16;
    const unsigned b0 = (unsigned)WS_HF + (rt0 * 112 + 28 * w) * 1024 + lane * 16;
    dgemm64_ring<28, 5, X_AUXB>(rs, a0, a0 + 112 * 1024, b0, b0 + 112 * 1024, (7 * rp2 + 3 * np2) % 28, acc);
  }
  STAMPT(ws, 2, 3);
  const int h = lane >> 5, r31 = lane & 31;
  const unsigned eoff = (unsigned)(((rt0 + (w & 1)) * 32 + r31) * D_ + (nt0 + (w >> 1)) * 32 + 16 * h) * 2;
  u32x4 xv[2];
#pragma unroll
  for (int c = 0; c < 2; ++c) xv[c] = *(const u32x4*)(x + eoff + 16 * c);
  __builtin_amdgcn_s_setprio(0);
  flag_wait<6>(sm, K2_FLG + 12, 8);
  {
    const int wl = 65536 + w * 16384;
    const char* wog = ws + WS_WO + (size_t)(nt0 * 32 + 8 * w) * 1024;
#pragma unroll
    for (int i = 0; i < 16; ++i) glds16(wog + (i >> 3) * 32768 + (i & 7) * 1024, lane * 16, lds_addr(sm) + wl + i * 1024);
    gu32* f = (gu32*)(ws + WS_OF + ((rt0 + ((lane >> 3) & 1)) * 32 + 8 * w + (lane & 7)) * 1024 + 1020);
    for (unsigned spins = 0; spins < (1u << 20); ++spins) {
      unsigned v = __hip_atomic_load(f, __ATOMIC_RELAXED, __HIP_MEMORY_SCOPE_AGENT);
      if (__all(v != ~0u)) break;
      __builtin_amdgcn_s_sleep(2);
    }
    asm volatile("" ::: "memory");
    STAMPT(ws, 2, 6);
    const unsigned b0 = (unsigned)WS_OF + (rt0 * 32 + 8 * w) * 1024 + lane * 16;
    u32x4 B[16];
    for (unsigned spins = 0; spins < (1u << 16); ++spins) {
      asm volatile("" ::: "memory");
#pragma unroll
      for (int i = 0; i < 16; ++i) B[i] = __builtin_amdgcn_raw_buffer_load_b128(rs, b0, (i & 1) * 32768 + (i >> 1) * 1024, 16);
      unsigned m = 0;
#pragma unroll
      for (int i = 0; i < 16; ++i) m = max(max(m, B[i][0]), max(B[i][1], max(B[i][2], B[i][3])));
      if (__builtin_expect(!__any(m == ~0u), 1)) break;
    }
    WAIT_VM(0);
#pragma unroll
    for (int ks = 0; ks < 8; ++ks) {
      const bf16x8 fa0 = lds_b128(sm, wl + ks * 1024 + lane * 16), fa1 = lds_b128(sm, wl + 8192 + ks * 1024 + lane * 16);
      const bf16x8 fb0 = __builtin_bit_cast(bf16x8, B[2 * ks]), fb1 = __builtin_bit_cast(bf16x8, B[2 * ks + 1]);
      acc[0] = MFMA(fa0, fb0, acc[0]);
      acc[2] = MFMA(fa1, fb0, acc[2]);
      acc[1] = MFMA(fa0, fb1, acc[1]);
      acc[3] = MFMA(fa1, fb1, acc[3]);
    }
  }
  {
    f32x4* red = (f32x4*)sm + lane;
#pragma unroll
    for (int t = 0; t < 4; ++t)
#pragma unroll
      for (int g = 0; g < 4; ++g)
        red[((t * 4 + w) * 4 + g) * 64] = f32x4{acc[t][4 * g], acc[t][4 * g + 1], acc[t][4 * g + 2], acc[t][4 * g + 3]};
    WAIT_LGKM0();
    lds_signal(sm, 4, lane);
    lds_wait(sm, 4, 4);
    const f32x4* rd = (const f32x4*)sm + r31;
    const __amdgpu_buffer_rsrc_t ro = __builtin_amdgcn_make_buffer_rsrc((void*)out, 0, S_ * D_ * 2, 0x00020000);
#pragma unroll
    for (int c = 0; c < 2; ++c) {
      const int g = 2 * h + c;
      f32x4 lo = rd[((w * 4 + 0) * 4 + g) * 64], hi = rd[((w * 4 + 0) * 4 + g) * 64 + 32];
#pragma unroll
      for (int src = 1; src < 4; ++src) {
        lo += rd[((w * 4 + src) * 4 + g) * 64];
        hi += rd[((w * 4 + src) * 4 + g) * 64 + 32];
      }
      f32x8 v;
      v[0] = lo[0] + bf_lo(xv[c][0]); v[1] = lo[1] + bf_hi(xv[c][0]); v[2] = lo[2] + bf_lo(xv[c][1]); v[3] = lo[3] + bf_hi(xv[c][1]);
      v[4] = hi[0] + bf_lo(xv[c][2]); v[5] = hi[1] + bf_hi(xv[c][2]); v[6] = hi[2] + bf_lo(xv[c][3]); v[7] = hi[3] + bf_hi(xv[c][3]);
#define X_OUTAUX 0
      __builtin_amdgcn_raw_buffer_store_b128(__builtin_bit_cast(u32x4, __builtin_convertvector(v, bf16x8)), ro, eoff + 16 * c, 0, X_OUTAUX);
    }
  }
  STAMPT(ws, 2, 4);
}

extern "C" __global__ __launch_bounds__(768) void k2_attn(const char* __restrict__ x, char* __restrict__ ws, char* __restrict__ out) {
  extern __shared__ __attribute__((aligned(16))) char sm[];
  const int lane = threadIdx.x & 63, wave = __builtin_amdgcn_readfirstlane(threadIdx.x >> 6);
  const int bid = blockIdx.x, xcd = bid & 7, idx = bid >> 3;
  if (threadIdx.x < 16) *(unsigned*)(sm + K2_FLG + threadIdx.x * 4) = 0u;
  const int head = xcd * 4 + (idx >> 3), qb = idx & 7, rt = qb * 8 + wave;
  if (wave >= 8) {
    k2_ffn_wave(sm, x, ws, out, wave - 8, lane, xcd, idx, head);
    return;
  }
  STAMPT(ws, 2, 0);
  const bf16x8 qf = *((const bf16x8*)(ws + WS_QF) + ((size_t)(head * 64 + rt) * 64 + lane));
  const int h = lane >> 5, r31 = lane & 31;
  const unsigned koff = lane * 16;
  const unsigned voff = 1024 + ((r31 < 16) ? vslot(r31, h) : 15) * 16;
  WAIT_LGKM0();
  BARRIER();
  f32x16 negm = {}, O = {};
#define EXP(v, i) v[i] = __builtin_amdgcn_exp2f(v[i])
#define TILE_STEP(SC, SN, OFFK2, OFFV0N, OFFV1)                                   \
  {                                                                               \
    SN = MFMA(kn, qf, negm);                                                      \
    SB();                                                                         \
    kn = lds_b128(kp, OFFK2);                                                     \
    EXP(SC, 0); EXP(SC, 1); EXP(SC, 2); EXP(SC, 3); EXP(SC, 4); EXP(SC, 5);       \
    SB();                                                                         \
    O = MFMA(v1p, p1p, O);                                                        \
    SB();                                                                         \
    v1p = lds_b128(vp, OFFV1);                                                    \
    EXP(SC, 6); EXP(SC, 7);                                                       \
    bf16x8 p0 = pack_lo(SC);                                                      \
    SB();                                                                         \
    EXP(SC, 8); EXP(SC, 9);                                                       \
    SB();                                                                         \
    O = MFMA(v0, p0, O);                                                          \
    SB();                                                                         \
    v0 = lds_b128(vp, OFFV0N);                                                    \
    EXP(SC, 10); EXP(SC, 11); EXP(SC, 12); EXP(SC, 13); EXP(SC, 14); EXP(SC, 15); \
    p1p = pack_hi(SC);                                                            \
    SB();                                                                         \
  }
#define TILE4()                                                        \
  TILE_STEP(sA, sB, 2 * KVTILE, 1 * KVTILE, 0 * KVTILE + VBLK);        \
  TILE_STEP(sB, sA, 3 * KVTILE, 2 * KVTILE, 1 * KVTILE + VBLK);        \
  TILE_STEP(sA, sB, 4 * KVTILE, 3 * KVTILE, 2 * KVTILE + VBLK);        \
  TILE_STEP(sB, sA, 5 * KVTILE, 4 * KVTILE, 3 * KVTILE + VBLK);        \
  kp += 4 * KVTILE; vp += 4 * KVTILE;
  {
    const char* kp = sm + koff;
    const char* vp = sm + voff;
#define X_SW 20
    lds_wait(sm, 0, 4);
    STAMPT(ws, 2, 1);
    const bf16x8 k0 = lds_b128(kp, 0);
    bf16x8 kn = lds_b128(kp, KVTILE), v0 = lds_b128(vp, 0), v1p = lds_b128(vp, VBLK), p1p = {};
    {
      const f32x16 z = {};
      f32x16 s0 = MFMA(k0, qf, z), s1 = MFMA(kn, qf, z);
      const float nm = -(xhalf_max(fmaxf(max16(s0), max16(s1))) + SM_DELTA);
#pragma unroll
      for (int e = 0; e < 16; ++e) negm[e] = nm;
    }
    f32x16 sA = MFMA(k0, qf, negm), sB;
    int ev = 4;
    for (int t = 0; t < 64; t += 4) {
      if (__builtin_expect(t == ev, 0)) {
        lds_wait(sm, t == 4 ? 1 : 2, 4);
        if (t != 4 && wave >= 4) __builtin_amdgcn_s_setprio(1);
        ev = (t == 4) ? X_SW : -100;
      }
      TILE4();
      asm volatile("" : "+v"(O), "+v"(sA), "+v"(p1p));
    }
    O = MFMA(v1p, p1p, O);
  }
  STAMPT(ws, 2, 2);
  float l = O[8];
  bool ok = (l < 0x1p120f);
#pragma unroll
  for (int e = 0; e < 8; ++e) ok = ok && (fabsf(O[e]) < 0x1p126f);
  if (!__all(ok)) {
    float m = -INFINITY;
#pragma unroll
    for (int e = 0; e < 16; ++e) O[e] = 0.f;
    for (int t = 0; t < 64; ++t) {
      f32x16 z = {};
      f32x16 sc = MFMA(lds_b128(sm, koff + t * KVTILE), qf, z);
      bf16x8 v0 = lds_b128(sm, voff + t * KVTILE), v1 = lds_b128(sm, voff + t * KVTILE + VBLK);
      float mn = fmaxf(m, xhalf_max(max16(sc)));
      float alpha = __builtin_amdgcn_exp2f(m - mn);
      m = mn;
#pragma unroll
      for (int e = 0; e < 16; ++e) { O[e] *= alpha; sc[e] = __builtin_amdgcn_exp2f(sc[e] - mn); }
      O = MFMA(v0, pack_lo(sc), O);
      O = MFMA(v1, pack_hi(sc), O);
    }
    l = O[8];
  }
  const float rl = __builtin_amdgcn_rcpf(l);
#pragma unroll
  for (int e = 0; e < 8; ++e) O[e] *= rl;
  u32x4 po = __builtin_bit_cast(u32x4, pack_lo(O));
#pragma unroll
  for (int e = 0; e < 4; ++e) po[e] = min(po[e], 0xFFFFFFFEu);
  __builtin_amdgcn_raw_buffer_store_b128(po, ws_rsrc(ws), (unsigned)WS_OF + (rt * 32 + head) * 1024 + lane * 16, 0, 16);
  WAIT_LGKM0();
  lds_signal(sm, 3, lane);
  STAMPT(ws, 2, 3);
}

extern "C" void kernel_launch(void* const* d_in, const int* in_sizes, int n_in, void* d_out, int out_size, void* d_ws,
                              size_t ws_size, hipStream_t stream) {
  const char* x = (const char*)d_in[0];
  const char* wqkv = (const char*)d_in[1];
  const char* wo = (const char*)d_in[2];
  const char* w1 = (const char*)d_in[4];
  const char* w2 = (const char*)d_in[5];
  char* ws = (char*)d_ws;
  (void)hipFuncSetAttribute((const void*)k1_qkv, hipFuncAttributeMaxDynamicSharedMemorySize, K1_LDS);
  (void)hipFuncSetAttribute((const void*)k2_attn, hipFuncAttributeMaxDynamicSharedMemorySize, K2_LDS);
  hipLaunchKernelGGL(k1_qkv, dim3(256), dim3(768), K1_LDS, stream, x, wqkv, w1, w2, wo, ws);
  hipLaunchKernelGGL(k2_attn, dim3(256), dim3(768), K2_LDS, stream, x, ws, (char*)d_out);
}
```

```cpp
#include <hip/hip_runtime.h>
#include <hip/hip_bf16.h>
#include <stdint.h>

typedef __attribute__((ext_vector_type(8))) __bf16 bf16x8;
typedef __attribute__((ext_vector_type(4))) __bf16 bf16x4;
typedef __attribute__((ext_vector_type(16))) float f32x16;
typedef __attribute__((ext_vector_type(8))) float f32x8;
typedef __attribute__((ext_vector_type(4))) float f32x4;
typedef __attribute__((ext_vector_type(2))) float f32x2;
typedef __attribute__((ext_vector_type(4))) unsigned u32x4;
typedef __attribute__((ext_vector_type(2))) unsigned u32x2;
#define LDS_AS __attribute__((address_space(3)))
#define DI __device__ __forceinline__
#define MFMA(a, b, c) __builtin_amdgcn_mfma_f32_32x32x16_bf16((a), (b), (c), 0, 0, 0)

constexpr int S_ = 2048, D_ = 512, FF_ = 1792, NH_ = 32;
constexpr int KVTILE = 2112;
constexpr int VBLK = 544;
constexpr int KVHEAD = 136 * 1024;
constexpr size_t WS_QF = 0;
constexpr size_t WS_KV = WS_QF + (size_t)2 * 1024 * 1024;
constexpr size_t WS_XN = WS_KV + (size_t)NH_ * KVHEAD;
constexpr size_t WS_W1 = WS_XN + (size_t)2 * 1024 * 1024;
constexpr size_t WS_W2 = WS_W1 + (size_t)1792 * 1024;
constexpr size_t WS_WO = WS_W2 + (size_t)1792 * 1024;
constexpr size_t WS_HF = WS_WO + (size_t)512 * 1024;
constexpr size_t WS_OF = WS_HF + (size_t)64 * 112 * 1024;
constexpr size_t WS_FLAG = WS_OF + (size_t)2 * 1024 * 1024;
constexpr size_t WS_END = WS_FLAG + 4096;

constexpr float QSCALE = 0.25f * 1.4426950408889634f;

DI unsigned lds_addr(const void* p) { return (unsigned)(uintptr_t)(LDS_AS const char*)p; }
DI void glds16(const void* sbase, unsigned voff, unsigned ldst) {
  unsigned keep;
  asm volatile("s_mov_b32 %0, m0\n\ts_mov_b32 m0, %3\n\ts_nop 0\n\tglobal_load_lds_dwordx4 %1, %2\n\ts_mov_b32 m0, %0"
               : "=&s"(keep) : "v"(voff), "s"(sbase), "s"(ldst) : "memory");
}
#define WAIT_VM(n) asm volatile("s_waitcnt vmcnt(" #n ")" ::: "memory")
#define WAIT_LGKM0() asm volatile("s_waitcnt lgkmcnt(0)" ::: "memory")
#define BARRIER() __builtin_amdgcn_s_barrier()
template <int N> DI void wait_vm() { asm volatile("s_waitcnt vmcnt(%0)" ::"i"(N) : "memory"); }

DI __amdgpu_buffer_rsrc_t ws_rsrc(const void* ws) { return __builtin_amdgcn_make_buffer_rsrc((void*)ws, 0, (int)WS_END, 0x00020000); }
template <typename T> DI void st16(__amdgpu_buffer_rsrc_t rs, unsigned off, T v) {
  __builtin_amdgcn_raw_buffer_store_b128(__builtin_bit_cast(u32x4, v), rs, off, 0, 16);
}
DI bf16x8 lds_b128(const char* sm, unsigned off) { return *(const bf16x8*)(sm + off); }
DI bf16x4 lds_tr(const char* sm, unsigned off) {
  return __builtin_amdgcn_ds_read_tr16_b64_v4bf16((LDS_AS bf16x4*)(LDS_AS char*)(sm + off));
}
DI bf16x8 cat8(bf16x4 lo, bf16x4 hi) { return __builtin_shufflevector(lo, hi, 0, 1, 2, 3, 4, 5, 6, 7); }
DI bf16x8 pack_lo(const f32x16& v) { return __builtin_convertvector(__builtin_shufflevector(v, v, 0, 1, 2, 3, 4, 5, 6, 7), bf16x8); }
DI bf16x8 pack_hi(const f32x16& v) { return __builtin_convertvector(__builtin_shufflevector(v, v, 8, 9, 10, 11, 12, 13, 14, 15), bf16x8); }
DI float bf_lo(unsigned u) { return __uint_as_float(u << 16); }
DI float bf_hi(unsigned u) { return __uint_as_float(u & 0xffff0000u); }
DI int vslot(int d, int h) {
  int s = (d < 4) ? d : (d >= 12) ? d - 8 : (d < 11) ? d + 4 : -1;
  return (s >= 0) ? (16 * h + s) : (32 + h);
}

DI LDS_AS unsigned* lds_word(char* sm, int off) { return (LDS_AS unsigned*)(LDS_AS char*)(sm + off); }
DI void flag_add(char* sm, int off, int lane) {
  if (lane == 0) __hip_atomic_fetch_add(lds_word(sm, off), 1u, __ATOMIC_RELAXED, __HIP_MEMORY_SCOPE_WORKGROUP);
  asm volatile("" ::: "memory");
}
DI unsigned flag_peek(char* sm, int off) { return __hip_atomic_load(lds_word(sm, off), __ATOMIC_RELAXED, __HIP_MEMORY_SCOPE_WORKGROUP); }
template <int SLEEP = 1>
DI void flag_wait(char* sm, int off, unsigned target) {
  while ((unsigned)__builtin_amdgcn_readfirstlane(flag_peek(sm, off)) < target) __builtin_amdgcn_s_sleep(SLEEP);
  asm volatile("" ::: "memory");
}

constexpr int K1_SLOT = 28672, K1_WOFF = 16384, K1_NSLOT = 5;
constexpr int K1_STATS = K1_SLOT * K1_NSLOT;
constexpr int K1_WPART = K1_STATS + 1024;
constexpr int K1_FLG = K1_WPART + 768;
constexpr int K1_SIDE = K1_FLG + 256;
constexpr int K1_LDS = K1_SIDE + 16384;
#define K1_READY(s) (K1_FLG + 4 * (s))
#define K1_DONE(s) (K1_FLG + 32 + 4 * (s))
#define K1_FIN (K1_FLG + 44)

template <int NV>
DI void k1_gemm_wave(char* sm, int w, int lane, int rg, int cg, char* ws) {
  const int h = lane >> 5, r31 = lane & 31;
  unsigned aoff[4];
  {
    const int row = 32 * w + r31;
#pragma unroll
    for (int ks = 0; ks < 4; ++ks) aoff[ks] = row * 128 + (((2 * ks + h) ^ ((row >> 1) & 7)) << 4);
  }
  const unsigned boff = K1_WOFF + (8 * h + ((lane & 15) >> 2)) * 192 + (16 * ((lane >> 4) & 1) + 4 * (lane & 3)) * 2;
  f32x16 acc[3] = {};
  bf16x8 fa[4], fb[4][3];
#pragma unroll
  for (int i = 2; i < 4; ++i) {
    fa[i] = bf16x8{};
#pragma unroll
    for (int j = 0; j < 3; ++j) fb[i][j] = bf16x8{};
  }
#define K1_MMA(ps)                                                        \
  _Pragma("unroll") for (int j = 0; j < 3; ++j) {                         \
    if (j >= 3 - NV) acc[j] = MFMA(fa[ps], fb[ps][j], acc[j]);   \
    else acc[j] = MFMA(fb[ps][j], fa[ps], acc[j]);               \
  }
  int slot = 0;
  unsigned rdy = 0;
#pragma nounroll
  for (int s = 0; s < 8; ++s) {
    if ((unsigned)__builtin_amdgcn_readfirstlane(rdy) < 4u) flag_wait(sm, K1_READY(s), 4);
    asm volatile("" ::: "memory");
    const char* sl = sm + slot;
#pragma unroll
    for (int ks = 0; ks < 4; ++ks) {
      if (ks == 2) rdy = flag_peek(sm, K1_READY(s + 1));
      fa[ks] = lds_b128(sl, aoff[ks]);
#pragma unroll
      for (int j = 0; j < 3; ++j)
        fb[ks][j] = cat8(lds_tr(sl, boff + ks * 16 * 192 + 64 * j), lds_tr(sl, boff + (ks * 16 + 4) * 192 + 64 * j));
      __builtin_amdgcn_sched_barrier(0);
      K1_MMA((ks + 2) & 3);
      __builtin_amdgcn_sched_barrier(0);
      if (ks == 1 && s >= 1 && s <= 3) flag_add(sm, K1_DONE(s - 1), lane);
    }
    slot += K1_SLOT;
    if (slot == K1_NSLOT * K1_SLOT) slot = 0;
  }
  K1_MMA(2);
  K1_MMA(3);
  asm volatile("" : "+v"(acc[0]), "+v"(acc[1]), "+v"(acc[2]));
  flag_wait(sm, K1_FIN, 4);
  const int rt = rg * 4 + w;
  const __amdgpu_buffer_rsrc_t rs = ws_rsrc(ws);
  const f32x2* stats = (const f32x2*)(sm + K1_STATS) + 32 * w;
  const float* wsum = (const float*)(sm + K1_WPART);
#pragma unroll
  for (int j = 0; j < 3; ++j) {
    const int ct = cg * 3 + j, role = ct >> 4, hp = (ct & 15) * 2;
    f32x16 v;
    if (j >= 3 - NV) {
      const float wsn = wsum[32 * j + r31] + wsum[96 + 32 * j + r31];
#pragma unroll
      for (int g = 0; g < 4; ++g) {
        f32x4 s0 = *(const f32x4*)(stats + 8 * g + 4 * h), s1 = *(const f32x4*)(stats + 8 * g + 4 * h + 2);
        v[4 * g + 0] = acc[j][4 * g + 0] * s0[0] + s0[1] * wsn;
        v[4 * g + 1] = acc[j][4 * g + 1] * s0[2] + s0[3] * wsn;
        v[4 * g + 2] = acc[j][4 * g + 2] * s1[0] + s1[1] * wsn;
        v[4 * g + 3] = acc[j][4 * g + 3] * s1[2] + s1[3] * wsn;
      }
      const unsigned base = (unsigned)WS_KV + (hp + (r31 >> 4)) * KVHEAD + rt * KVTILE + 1024;
      const int sl = vslot(r31 & 15, h);
      st16(rs, base + sl * 16, pack_lo(v));
      st16(rs, base + VBLK + sl * 16, pack_hi(v));
      if ((lane & 15) == 0)
        st16(rs, base + h * VBLK + 15 * 16, u32x4{0x3f803f80u, 0x3f803f80u, 0x3f803f80u, 0x3f803f80u});
    } else {
      f32x2 st = stats[r31];
      if (role == 0) { st[0] *= QSCALE; st[1] *= QSCALE; }
#pragma unroll
      for (int g = 0; g < 4; ++g) {
        f32x4 wsv = *(const f32x4*)(wsum + 32 * j + 8 * g + 4 * h) + *(const f32x4*)(wsum + 96 + 32 * j + 8 * g + 4 * h);
#pragma unroll
        for (int e = 0; e < 4; ++e) v[4 * g + e] = acc[j][4 * g + e] * st[0] + st[1] * wsv[e];
      }
      if (role == 0) {
        const unsigned base = (unsigned)WS_QF + (hp * 64 + rt) * 1024 + lane * 16;
        st16(rs, base, pack_lo(v));
        st16(rs, base + 64 * 1024, pack_hi(v));
      } else {
        const unsigned base = (unsigned)WS_KV + hp * KVHEAD + rt * KVTILE + lane * 16;
        st16(rs, base, pack_lo(v));
        st16(rs, base + KVHEAD, pack_hi(v));
      }
    }
  }
}


#define MFMA16(a, b, c) __builtin_amdgcn_mfma_f32_16x16x32_bf16((a), (b), (c), 0, 0, 0)
DI void k1_stats_wave(char* sm, const char* __restrict__ xg, int hw, int lane, int rg, int cg, char* ws) {
  const int r15 = lane & 15, kq = lane >> 4;
  unsigned aoff[4];
#pragma unroll
  for (int i = 0; i < 4; ++i) {
    const int row = 32 * hw + 16 * (i >> 1) + r15, c = 4 * (i & 1) + kq;
    aoff[i] = row * 128 + ((c ^ ((row >> 1) & 7)) << 4);
  }
  const unsigned boff = K1_WOFF + (32 * (hw & 1) + 8 * kq + (r15 >> 2)) * 192 + (48 * (hw >> 1) + 4 * (lane & 3)) * 2;
  const bf16x8 ones = __builtin_bit_cast(bf16x8, u32x4{0x3f803f80u, 0x3f803f80u, 0x3f803f80u, 0x3f803f80u});
  f32x4 gram[2] = {}, rsum[2] = {}, wacc[3] = {};
  bf16x8 xa[2][4] = {}, wb[2][3] = {};
  int slot = 0;
  u32x4 xr2[2];
#pragma unroll
  for (int i = 0; i < 2; ++i)
    xr2[i] = *(const u32x4*)(xg + (size_t)(rg * 128 + cg * 8 + (lane & 7)) * 1024 + ((lane >> 3) + 8 * (2 * hw + i)) * 16);
  auto math = [&](const bf16x8 (&xp)[4], const bf16x8 (&wp)[3]) {
#pragma unroll
    for (int i = 0; i < 4; ++i) {
      gram[i >> 1] = MFMA16(xp[i], xp[i], gram[i >> 1]);
      rsum[i >> 1] = MFMA16(xp[i], ones, rsum[i >> 1]);
    }
#pragma unroll
    for (int j = 0; j < 3; ++j) wacc[j] = MFMA16(ones, wp[j], wacc[j]);
  };
#pragma nounroll
  for (int s2 = 0; s2 < 4; ++s2) {
#pragma unroll
    for (int par = 0; par < 2; ++par) {
      flag_wait(sm, K1_READY(2 * s2 + par), 4);
      const char* sl = sm + slot;
#pragma unroll
      for (int i = 0; i < 4; ++i) xa[par][i] = lds_b128(sl, aoff[i]);
#pragma unroll
      for (int j = 0; j < 3; ++j) wb[par][j] = cat8(lds_tr(sl, boff + 32 * j), lds_tr(sl, boff + 4 * 192 + 32 * j));
      __builtin_amdgcn_sched_barrier(0);
      math(xa[par ^ 1], wb[par ^ 1]);
      __builtin_amdgcn_sched_barrier(0);
      WAIT_LGKM0();
      if (2 * s2 + par <= 2) flag_add(sm, K1_DONE(2 * s2 + par), lane);
      slot += K1_SLOT;
      if (slot == K1_NSLOT * K1_SLOT) slot = 0;
    }
  }
  math(xa[1], wb[1]);
  if (lane < 16) {
#pragma unroll
    for (int j = 0; j < 3; ++j) *(float*)(sm + K1_WPART + ((hw & 1) * 96 + 48 * (hw >> 1) + 16 * j + lane) * 4) = wacc[j][0];
  }
  if ((r15 >> 2) == kq) {
#pragma unroll
    for (int g2 = 0; g2 < 2; ++g2) {
      const int e = r15 & 3;
      const float sq = e == 0 ? gram[g2][0] : e == 1 ? gram[g2][1] : e == 2 ? gram[g2][2] : gram[g2][3];
      const float sum = e == 0 ? rsum[g2][0] : e == 1 ? rsum[g2][1] : e == 2 ? rsum[g2][2] : rsum[g2][3];
      const int row = 32 * hw + 16 * g2 + r15;
      const float mean = sum * (1.f / 512.f);
      float var = sq * (1.f / 512.f) - mean * mean;
      if (__builtin_expect(!(var > 1e-3f * mean * mean), 0)) {
        const unsigned* xr = (const unsigned*)(xg + (size_t)(rg * 128 + row) * 1024);
        float acc = 0.f;
        for (int i = 0; i < 256; ++i) {
          const unsigned u = xr[i];
          const float a = bf_lo(u) - mean, b = bf_hi(u) - mean;
          acc = fmaf(a, a, fmaf(b, b, acc));
        }
        var = acc * (1.f / 512.f);
      }
      const float rstd = __builtin_amdgcn_rsqf(var + 1e-5f);
      *(f32x2*)(sm + K1_STATS + row * 8) = f32x2{rstd, -mean * rstd};
    }
  }
  WAIT_LGKM0();
  flag_add(sm, K1_FIN, lane);
  flag_wait(sm, K1_FIN, 4);
  {
    const int p = lane & 7, lrow = cg * 8 + p;
    const f32x2 st = *(const f32x2*)(sm + K1_STATS + lrow * 8);
    const int grow = rg * 128 + lrow;
#pragma unroll
    for (int i = 0; i < 2; ++i) {
      const int c = (lane >> 3) + 8 * (2 * hw + i);
      const u32x4 u = xr2[i];
      f32x8 v;
#pragma unroll
      for (int e = 0; e < 4; ++e) { v[2 * e] = bf_lo(u[e]) * st[0] + st[1]; v[2 * e + 1] = bf_hi(u[e]) * st[0] + st[1]; }
      st16(ws_rsrc(ws), (unsigned)WS_XN + (((grow >> 5) * 32 + (c >> 1)) * 64 + (c & 1) * 32 + (grow & 31)) * 16,
           __builtin_convertvector(v, bf16x8));
    }
  }
}

DI void k1_loader_wave(char* sm, unsigned sbase, const char* xg, const char* wqkv, const char* w1, const char* w2,
                       const char* wo, int lw, int lane, int bid, int rg, int cg, int rot, char* ws) {
  unsigned xoff[4], woff[3];
#pragma unroll
  for (int p = 0; p < 4; ++p) {
    int c = (lane & 7) ^ ((4 * p + (lane >> 4)) & 7);
    xoff[p] = (unsigned)((rg * 128 + 32 * lw + 8 * p + (lane >> 3)) * 1024 + c * 16);
  }
#pragma unroll
  for (int i = 0; i < 3; ++i) {
    int idx = 64 * (3 * lw + i) + lane;
    int row = idx / 12, ch = idx - row * 12;
    woff[i] = (unsigned)(row * 3072 + cg * 192 + ch * 16);
  }
  auto issue = [&](int s, int slot) {
    const int ks = (s + rot) & 7;
    const unsigned dst = sbase + slot;
#pragma unroll
    for (int i = 0; i < 4; ++i) glds16(xg + ks * 128, xoff[i], dst + lw * 4096 + i * 1024);
#pragma unroll
    for (int i = 0; i < 3; ++i) glds16(wqkv + (size_t)ks * 64 * 3072, woff[i], dst + K1_WOFF + lw * 3072 + i * 1024);
  };
  issue(0, 0);
  issue(1, K1_SLOT);
  const char* swsrc; int sld, kb, nb, sks; size_t sdst; bool perm;
  if (bid < 112) { swsrc = w1; sld = FF_ * 2; kb = bid / 14; nb = bid - kb * 14; sks = 32; sdst = WS_W1; perm = false; }
  else if (bid < 224) { int v = bid - 112; swsrc = w2; sld = D_ * 2; kb = v >> 2; nb = v & 3; sks = 112; sdst = WS_W2; perm = true; }
  else { int v = bid - 224; swsrc = wo; sld = D_ * 2; kb = v >> 2; nb = v & 3; sks = 32; sdst = WS_WO; perm = true; }
  const int h = lane >> 5, q = (lane & 15) >> 2;
  const unsigned cb = (16 * ((lane >> 4) & 1) + 4 * (lane & 3)) * 2;
  {
    int islot = 2 * K1_SLOT;
#pragma nounroll
    for (int s = 2; s < 8; ++s) {
      wait_vm<7>(); flag_add(sm, K1_READY(s - 2), lane);
      if (s >= 5) flag_wait(sm, K1_DONE(s - 5), 8);
      issue(s, islot);
      islot += K1_SLOT;
      if (islot == K1_NSLOT * K1_SLOT) islot = 0;
    }
  }
  wait_vm<7>(); flag_add(sm, K1_READY(6), lane);
#pragma unroll
  for (int i = 0; i < 4; ++i) {
    const int p = 4 * lw + i;
    glds16(swsrc + (size_t)(kb * 64 + 4 * p) * sld + nb * 256, (unsigned)((lane >> 4) * sld + (lane & 15) * 16),
           sbase + K1_SIDE + p * 1024);
  }
  wait_vm<4>(); flag_add(sm, K1_READY(7), lane);
  WAIT_VM(0);
  {
    const int r0 = perm ? 4 * h : 8 * h, r1 = perm ? 8 + 4 * h : 8 * h + 4;
    const unsigned o0 = K1_SIDE + (16 * lw + r0 + q) * 256 + cb, o1 = K1_SIDE + (16 * lw + r1 + q) * 256 + cb;
#pragma unroll
    for (int nt = 0; nt < 4; ++nt) {
      bf16x8 f = cat8(lds_tr(sm, o0 + nt * 64), lds_tr(sm, o1 + nt * 64));
      st16(ws_rsrc(ws), (unsigned)sdst + ((nb * 4 + nt) * sks + kb * 4 + lw) * 1024 + lane * 16, f);
    }
  }
}

extern "C" __global__ __launch_bounds__(768) void k1_qkv(const char* __restrict__ x, const char* __restrict__ wqkv,
                                              const char* __restrict__ w1, const char* __restrict__ w2,
                                              const char* __restrict__ wo, char* __restrict__ ws) {
  extern __shared__ __attribute__((aligned(16))) char sm[];
  const int lane = threadIdx.x & 63, wave = __builtin_amdgcn_readfirstlane(threadIdx.x >> 6);
  const int bid = blockIdx.x, xcd = bid & 7, idx = bid >> 3;
  const int rg = 4 * (xcd >> 1) + (idx & 3), cg = 8 * (xcd & 1) + (idx >> 2);
  if (threadIdx.x >= 256 && threadIdx.x < 272) *(unsigned*)(sm + K1_FLG + (threadIdx.x - 256) * 4) = 0u;
  WAIT_LGKM0();
  BARRIER();
  if (wave < 4) {
    const int rot = ((idx >> 2) + 2 * (idx & 3)) & 7;
    k1_loader_wave(sm, lds_addr(sm), x, wqkv, w1, w2, wo, wave, lane, bid, rg, cg, rot, ws);
  } else if (wave < 8) {
    if (cg < 10) k1_gemm_wave<0>(sm, wave - 4, lane, rg, cg, ws);
    else if (cg == 10) k1_gemm_wave<1>(sm, wave - 4, lane, rg, cg, ws);
    else k1_gemm_wave<3>(sm, wave - 4, lane, rg, cg, ws);
  } else {
    if (bid == 0) st16(ws_rsrc(ws), (unsigned)WS_FLAG + (wave - 8) * 1024 + lane * 16, u32x4{0u, 0u, 0u, 0u});
#pragma unroll
    for (int i = 0; i < 2; ++i)
      st16(ws_rsrc(ws), (unsigned)WS_OF + bid * 8192 + (wave - 8) * 2048 + i * 1024 + lane * 16, u32x4{~0u, ~0u, ~0u, ~0u});
    k1_stats_wave(sm, x, wave - 8, lane, rg, cg, ws);
  }
}

constexpr int K2_FLG = KVHEAD;
constexpr int K2_LDS = K2_FLG + 64;
constexpr float SM_DELTA = 80.f;
typedef __attribute__((address_space(1))) unsigned gu32;
#define SB() __builtin_amdgcn_sched_barrier(0)

DI float xhalf_max(float v) {
  auto r = __builtin_amdgcn_permlane32_swap(__float_as_uint(v), __float_as_uint(v), false, false);
  return fmaxf(__uint_as_float(r[0]), __uint_as_float(r[1]));
}
DI float max16(const f32x16& s) {
  float m01 = fmaxf(fmaxf(s[0], s[1]), s[2]), m23 = fmaxf(fmaxf(s[3], s[4]), s[5]);
  float m45 = fmaxf(fmaxf(s[6], s[7]), s[8]), m67 = fmaxf(fmaxf(s[9], s[10]), s[11]);
  float m89 = fmaxf(fmaxf(s[12], s[13]), s[14]);
  return fmaxf(fmaxf(fmaxf(m01, m23), m45), fmaxf(fmaxf(m67, m89), s[15]));
}
DI void lds_signal(char* sm, int word, int lane) { flag_add(sm, K2_FLG + 4 * word, lane); }
DI void lds_wait(char* sm, int word, unsigned target) { flag_wait(sm, K2_FLG + 4 * word, target); }

template <int NKS, int PF, int AUXB>
DI void dgemm64_ring(__amdgpu_buffer_rsrc_t rs, unsigned a0, unsigned a1, unsigned b0, unsigned b1, int rot, f32x16 (&acc)[4]) {
  u32x4 A0[PF], A1[PF], B0[PF], B1[PF];
  int ko = rot * 1024;
#pragma unroll
  for (int kk = 0; kk < NKS + PF - 1; ++kk) {
    if (kk < NKS) {
      const int sl = kk % PF;
      A0[sl] = __builtin_amdgcn_raw_buffer_load_b128(rs, a0, ko, 0);
      B0[sl] = __builtin_amdgcn_raw_buffer_load_b128(rs, b0, ko, AUXB);
      A1[sl] = __builtin_amdgcn_raw_buffer_load_b128(rs, a1, ko, 0);
      B1[sl] = __builtin_amdgcn_raw_buffer_load_b128(rs, b1, ko, AUXB);
      ko += 1024;
      if (ko == NKS * 1024) ko = 0;
    }
    SB();
    if (kk >= PF - 1) {
      const int sl = (kk - (PF - 1)) % PF;
      const bf16x8 fa0 = __builtin_bit_cast(bf16x8, A0[sl]), fa1 = __builtin_bit_cast(bf16x8, A1[sl]);
      const bf16x8 fb0 = __builtin_bit_cast(bf16x8, B0[sl]), fb1 = __builtin_bit_cast(bf16x8, B1[sl]);
      acc[0] = MFMA(fa0, fb0, acc[0]);
      acc[2] = MFMA(fa1, fb0, acc[2]);
      acc[1] = MFMA(fa0, fb1, acc[1]);
      acc[3] = MFMA(fa1, fb1, acc[3]);
    }
    SB();
  }
  asm volatile("" : "+v"(acc[0]), "+v"(acc[1]), "+v"(acc[2]), "+v"(acc[3]));
}

DI void k2_ffn_wave(char* sm, const char* __restrict__ x, char* __restrict__ ws, char* __restrict__ out, int w, int lane, int panel, int ci, int head) {
  {
    const unsigned sb = lds_addr(sm) + w * 1024, dl = lane * 16;
    const char* kvg = ws + WS_KV + (size_t)head * KVHEAD + w * 1024;
#pragma unroll
    for (int i = 0; i < 8; ++i) glds16(kvg + i * 4096, dl, sb + i * 4096);
    WAIT_LGKM0();
    BARRIER();
    WAIT_VM(4);
    lds_signal(sm, 0, lane);
#pragma unroll
    for (int i = 8; i < 16; ++i) glds16(kvg + i * 4096, dl, sb + i * 4096);
    WAIT_VM(4);
    lds_signal(sm, 1, lane);
#pragma unroll
    for (int i = 16; i < 34; ++i) glds16(kvg + i * 4096, dl, sb + i * 4096);
    WAIT_VM(0);
    lds_signal(sm, 2, lane);
  }
  __builtin_amdgcn_s_setprio(2);
  const __amdgpu_buffer_rsrc_t rs = __builtin_amdgcn_make_buffer_rsrc((void*)ws, 0, (int)WS_END, 0x00020000);
  gu32* flags = (gu32*)(ws + WS_FLAG) + panel * 128;
  if (ci < 28) {
    const int rt0 = panel * 8 + 2 * w, nt0 = 2 * ci;
    const unsigned a0 = (unsigned)WS_W1 + nt0 * 32768 + lane * 16, b0 = (unsigned)WS_XN + rt0 * 32768 + lane * 16;
    f32x16 acc[4] = {};
    dgemm64_ring<32, 5, 0>(rs, a0, a0 + 32768, b0, b0 + 32768, ci, acc);
#pragma unroll
    for (int i = 0; i < 2; ++i)
#pragma unroll
      for (int j = 0; j < 2; ++j) {
        f32x16 v = acc[2 * i + j];
#pragma unroll
        for (int e = 0; e < 16; ++e) v[e] = fmaxf(v[e], 0.f);
        const unsigned dst = (unsigned)WS_HF + ((rt0 + j) * 112 + (nt0 + i) * 2) * 1024 + lane * 16;
        __builtin_amdgcn_raw_buffer_store_b128(__builtin_bit_cast(u32x4, pack_lo(v)), rs, dst, 0, 16);
        __builtin_amdgcn_raw_buffer_store_b128(__builtin_bit_cast(u32x4, pack_hi(v)), rs, dst + 1024, 0, 16);
      }
    WAIT_VM(0);
    if (lane == 0) __hip_atomic_store(flags + w * 32 + ci, 1u, __ATOMIC_RELAXED, __HIP_MEMORY_SCOPE_AGENT);
  }
  const int rp2 = ci & 3, np2 = ci >> 2;
  {
    __builtin_amdgcn_s_setprio(0);
    gu32* f = flags + rp2 * 32 + 7 * w + min(lane, 6);
    for (unsigned spins = 0; spins < (1u << 20); ++spins) {
      unsigned v = __hip_atomic_load(f, __ATOMIC_RELAXED, __HIP_MEMORY_SCOPE_AGENT);
      if (__all(v == 1u)) break;
      __builtin_amdgcn_s_sleep(8);
    }
    __builtin_amdgcn_fence(__ATOMIC_ACQUIRE, "wavefront");
    asm volatile("" ::: "memory");
  }
  __builtin_amdgcn_s_setprio(2);
  const int rt0 = panel * 8 + 2 * rp2, nt0 = 2 * np2;
  f32x16 acc[4] = {};
  {
    const unsigned a0 = (unsigned)WS_W2 + (nt0 * 112 + 28 * w) * 1024 + lane * 16;
    const unsigned b0 = (unsigned)WS_HF + (rt0 * 112 + 28 * w) * 1024 + lane * 16;
    dgemm64_ring<28, 5, 16>(rs, a0, a0 + 112 * 1024, b0, b0 + 112 * 1024, (7 * rp2 + 3 * np2) % 28, acc);
  }
  const int h = lane >> 5, r31 = lane & 31;
  const unsigned eoff = (unsigned)(((rt0 + (w & 1)) * 32 + r31) * D_ + (nt0 + (w >> 1)) * 32 + 16 * h) * 2;
  u32x4 xv[2];
#pragma unroll
  for (int c = 0; c < 2; ++c) xv[c] = *(const u32x4*)(x + eoff + 16 * c);
  __builtin_amdgcn_s_setprio(0);
  flag_wait<6>(sm, K2_FLG + 12, 8);
  {
    const int wl = 65536 + w * 16384;
    const char* wog = ws + WS_WO + (size_t)(nt0 * 32 + 8 * w) * 1024;
#pragma unroll
    for (int i = 0; i < 16; ++i) glds16(wog + (i >> 3) * 32768 + (i & 7) * 1024, lane * 16, lds_addr(sm) + wl + i * 1024);
    gu32* f = (gu32*)(ws + WS_OF + ((rt0 + ((lane >> 3) & 1)) * 32 + 8 * w + (lane & 7)) * 1024 + 1020);
    for (unsigned spins = 0; spins < (1u << 20); ++spins) {
      unsigned v = __hip_atomic_load(f, __ATOMIC_RELAXED, __HIP_MEMORY_SCOPE_AGENT);
      if (__all(v != ~0u)) break;
      __builtin_amdgcn_s_sleep(2);
    }
    asm volatile("" ::: "memory");
    const unsigned b0 = (unsigned)WS_OF + (rt0 * 32 + 8 * w) * 1024 + lane * 16;
    u32x4 B[16];
    for (unsigned spins = 0; spins < (1u << 16); ++spins) {
      asm volatile("" ::: "memory");
#pragma unroll
      for (int i = 0; i < 16; ++i) B[i] = __builtin_amdgcn_raw_buffer_load_b128(rs, b0, (i & 1) * 32768 + (i >> 1) * 1024, 16);
      unsigned m = 0;
#pragma unroll
      for (int i = 0; i < 16; ++i) m = max(max(m, B[i][0]), max(B[i][1], max(B[i][2], B[i][3])));
      if (__builtin_expect(!__any(m == ~0u), 1)) break;
    }
    WAIT_VM(0);
#pragma unroll
    for (int ks = 0; ks < 8; ++ks) {
      const bf16x8 fa0 = lds_b128(sm, wl + ks * 1024 + lane * 16), fa1 = lds_b128(sm, wl + 8192 + ks * 1024 + lane * 16);
      const bf16x8 fb0 = __builtin_bit_cast(bf16x8, B[2 * ks]), fb1 = __builtin_bit_cast(bf16x8, B[2 * ks + 1]);
      acc[0] = MFMA(fa0, fb0, acc[0]);
      acc[2] = MFMA(fa1, fb0, acc[2]);
      acc[1] = MFMA(fa0, fb1, acc[1]);
      acc[3] = MFMA(fa1, fb1, acc[3]);
    }
  }
  {
    f32x4* red = (f32x4*)sm + lane;
#pragma unroll
    for (int t = 0; t < 4; ++t)
#pragma unroll
      for (int g = 0; g < 4; ++g)
        red[((t * 4 + w) * 4 + g) * 64] = f32x4{acc[t][4 * g], acc[t][4 * g + 1], acc[t][4 * g + 2], acc[t][4 * g + 3]};
    WAIT_LGKM0();
    lds_signal(sm, 4, lane);
    lds_wait(sm, 4, 4);
    const f32x4* rd = (const f32x4*)sm + r31;
    const __amdgpu_buffer_rsrc_t ro = __builtin_amdgcn_make_buffer_rsrc((void*)out, 0, S_ * D_ * 2, 0x00020000);
#pragma unroll
    for (int c = 0; c < 2; ++c) {
      const int g = 2 * h + c;
      f32x4 lo = rd[((w * 4 + 0) * 4 + g) * 64], hi = rd[((w * 4 + 0) * 4 + g) * 64 + 32];
#pragma unroll
      for (int src = 1; src < 4; ++src) {
        lo += rd[((w * 4 + src) * 4 + g) * 64];
        hi += rd[((w * 4 + src) * 4 + g) * 64 + 32];
      }
      f32x8 v;
      v[0] = lo[0] + bf_lo(xv[c][0]); v[1] = lo[1] + bf_hi(xv[c][0]); v[2] = lo[2] + bf_lo(xv[c][1]); v[3] = lo[3] + bf_hi(xv[c][1]);
      v[4] = hi[0] + bf_lo(xv[c][2]); v[5] = hi[1] + bf_hi(xv[c][2]); v[6] = hi[2] + bf_lo(xv[c][3]); v[7] = hi[3] + bf_hi(xv[c][3]);
      __builtin_amdgcn_raw_buffer_store_b128(__builtin_bit_cast(u32x4, __builtin_convertvector(v, bf16x8)), ro, eoff + 16 * c, 0, 0);
    }
  }
}

extern "C" __global__ __launch_bounds__(768) void k2_attn(const char* __restrict__ x, char* __restrict__ ws, char* __restrict__ out) {
  extern __shared__ __attribute__((aligned(16))) char sm[];
  const int lane = threadIdx.x & 63, wave = __builtin_amdgcn_readfirstlane(threadIdx.x >> 6);
  const int bid = blockIdx.x, xcd = bid & 7, idx = bid >> 3;
  if (threadIdx.x < 16) *(unsigned*)(sm + K2_FLG + threadIdx.x * 4) = 0u;
  const int head = xcd * 4 + (idx >> 3), qb = idx & 7, rt = qb * 8 + wave;
  if (wave >= 8) {
    k2_ffn_wave(sm, x, ws, out, wave - 8, lane, xcd, idx, head);
    return;
  }
  const bf16x8 qf = *((const bf16x8*)(ws + WS_QF) + ((size_t)(head * 64 + rt) * 64 + lane));
  const int h = lane >> 5, r31 = lane & 31;
  const unsigned koff = lane * 16;
  const unsigned voff = 1024 + ((r31 < 16) ? vslot(r31, h) : 15) * 16;
  WAIT_LGKM0();
  BARRIER();
  f32x16 negm = {}, O = {};
#define EXP(v, i) v[i] = __builtin_amdgcn_exp2f(v[i])
#define TILE_STEP(SC, SN, OFFK2, OFFV0N, OFFV1)                                   \
  {                                                                               \
    SN = MFMA(kn, qf, negm);                                                      \
    SB();                                                                         \
    kn = lds_b128(kp, OFFK2);                                                     \
    EXP(SC, 0); EXP(SC, 1); EXP(SC, 2); EXP(SC, 3); EXP(SC, 4); EXP(SC, 5);       \
    SB();                                                                         \
    O = MFMA(v1p, p1p, O);                                                        \
    SB();                                                                         \
    v1p = lds_b128(vp, OFFV1);                                                    \
    EXP(SC, 6); EXP(SC, 7);                                                       \
    bf16x8 p0 = pack_lo(SC);                                                      \
    SB();                                                                         \
    EXP(SC, 8); EXP(SC, 9);                                                       \
    SB();                                                                         \
    O = MFMA(v0, p0, O);                                                          \
    SB();                                                                         \
    v0 = lds_b128(vp, OFFV0N);                                                    \
    EXP(SC, 10); EXP(SC, 11); EXP(SC, 12); EXP(SC, 13); EXP(SC, 14); EXP(SC, 15); \
    p1p = pack_hi(SC);                                                            \
    SB();                                                                         \
  }
#define TILE4()                                                        \
  TILE_STEP(sA, sB, 2 * KVTILE, 1 * KVTILE, 0 * KVTILE + VBLK);        \
  TILE_STEP(sB, sA, 3 * KVTILE, 2 * KVTILE, 1 * KVTILE + VBLK);        \
  TILE_STEP(sA, sB, 4 * KVTILE, 3 * KVTILE, 2 * KVTILE + VBLK);        \
  TILE_STEP(sB, sA, 5 * KVTILE, 4 * KVTILE, 3 * KVTILE + VBLK);        \
  kp += 4 * KVTILE; vp += 4 * KVTILE;
  {
    const char* kp = sm + koff;
    const char* vp = sm + voff;
    lds_wait(sm, 0, 4);
    const bf16x8 k0 = lds_b128(kp, 0);
    bf16x8 kn = lds_b128(kp, KVTILE), v0 = lds_b128(vp, 0), v1p = lds_b128(vp, VBLK), p1p = {};
    {
      const f32x16 z = {};
      f32x16 s0 = MFMA(k0, qf, z), s1 = MFMA(kn, qf, z);
      const float nm = -(xhalf_max(fmaxf(max16(s0), max16(s1))) + SM_DELTA);
#pragma unroll
      for (int e = 0; e < 16; ++e) negm[e] = nm;
    }
    f32x16 sA = MFMA(k0, qf, negm), sB;
    int ev = 4;
    for (int t = 0; t < 64; t += 4) {
      if (__builtin_expect(t == ev, 0)) {
        lds_wait(sm, t == 4 ? 1 : 2, 4);
        if (t != 4 && wave >= 4) __builtin_amdgcn_s_setprio(1);
        ev = (t == 4) ? 20 : -100;
      }
      TILE4();
      asm volatile("" : "+v"(O), "+v"(sA), "+v"(p1p));
    }
    O = MFMA(v1p, p1p, O);
  }
  float l = O[8];
  bool ok = (l < 0x1p120f);
  float omax = 0.f;
#pragma unroll
  for (int e = 0; e < 8; ++e) { ok = ok && (fabsf(O[e]) < 0x1p126f); omax = fmaxf(omax, fabsf(O[e])); }
  ok = ok && (omax > 0x1p-100f);
  if (!__all(ok)) {
    float m = -INFINITY;
#pragma unroll
    for (int e = 0; e < 16; ++e) O[e] = 0.f;
    for (int t = 0; t < 64; ++t) {
      f32x16 z = {};
      f32x16 sc = MFMA(lds_b128(sm, koff + t * KVTILE), qf, z);
      bf16x8 v0 = lds_b128(sm, voff + t * KVTILE), v1 = lds_b128(sm, voff + t * KVTILE + VBLK);
      float mn = fmaxf(m, xhalf_max(max16(sc)));
      float alpha = __builtin_amdgcn_exp2f(m - mn);
      m = mn;
#pragma unroll
      for (int e = 0; e < 16; ++e) { O[e] *= alpha; sc[e] = __builtin_amdgcn_exp2f(sc[e] - mn); }
      O = MFMA(v0, pack_lo(sc), O);
      O = MFMA(v1, pack_hi(sc), O);
    }
    l = O[8];
  }
  const float rl = __builtin_amdgcn_rcpf(l);
#pragma unroll
  for (int e = 0; e < 8; ++e) O[e] *= rl;
  u32x4 po = __builtin_bit_cast(u32x4, pack_lo(O));
#pragma unroll
  for (int e = 0; e < 4; ++e) po[e] = min(po[e], 0xFFFFFFFEu);
  __builtin_amdgcn_raw_buffer_store_b128(po, ws_rsrc(ws), (unsigned)WS_OF + (rt * 32 + head) * 1024 + lane * 16, 0, 16);
  WAIT_LGKM0();
  lds_signal(sm, 3, lane);
}

extern "C" void kernel_launch(void* const* d_in, const int* in_sizes, int n_in, void* d_out, int out_size, void* d_ws,
                              size_t ws_size, hipStream_t stream) {
  const char* x = (const char*)d_in[0];
  const char* wqkv = (const char*)d_in[1];
  const char* wo = (const char*)d_in[2];
  const char* w1 = (const char*)d_in[4];
  const char* w2 = (const char*)d_in[5];
  char* ws = (char*)d_ws;
  (void)hipFuncSetAttribute((const void*)k1_qkv, hipFuncAttributeMaxDynamicSharedMemorySize, K1_LDS);
  (void)hipFuncSetAttribute((const void*)k2_attn, hipFuncAttributeMaxDynamicSharedMemorySize, K2_LDS);
  hipLaunchKernelGGL(k1_qkv, dim3(256), dim3(768), K1_LDS, stream, x, wqkv, w1, w2, wo, ws);
  hipLaunchKernelGGL(k2_attn, dim3(256), dim3(768), K2_LDS, stream, x, ws, (char*)d_out);
}
```

```cpp
#include <hip/hip_runtime.h>
#include <hip/hip_bf16.h>
#include <stdint.h>

typedef __attribute__((ext_vector_type(8))) __bf16 bf16x8;
typedef __attribute__((ext_vector_type(4))) __bf16 bf16x4;
typedef __attribute__((ext_vector_type(16))) float f32x16;
typedef __attribute__((ext_vector_type(8))) float f32x8;
typedef __attribute__((ext_vector_type(4))) float f32x4;
typedef __attribute__((ext_vector_type(2))) float f32x2;
typedef __attribute__((ext_vector_type(4))) unsigned u32x4;
typedef __attribute__((ext_vector_type(2))) unsigned u32x2;
#define LDS_AS __attribute__((address_space(3)))
#define DI __device__ __forceinline__
#define MFMA(a, b, c) __builtin_amdgcn_mfma_f32_32x32x16_bf16((a), (b), (c), 0, 0, 0)

constexpr int S_ = 2048, D_ = 512, FF_ = 1792, NH_ = 32;
constexpr int KVTILE = 2112;
constexpr int VBLK = 544;
constexpr int KVHEAD = 136 * 1024;
constexpr size_t WS_QF = 0;
constexpr size_t WS_KV = WS_QF + (size_t)2 * 1024 * 1024;
constexpr size_t WS_XN = WS_KV + (size_t)NH_ * KVHEAD;
constexpr size_t WS_W1 = WS_XN + (size_t)2 * 1024 * 1024;
constexpr size_t WS_W2 = WS_W1 + (size_t)1792 * 1024;
constexpr size_t WS_WO = WS_W2 + (size_t)1792 * 1024;
constexpr size_t WS_HF = WS_WO + (size_t)512 * 1024;
constexpr size_t WS_OF = WS_HF + (size_t)64 * 112 * 1024;
constexpr size_t WS_FLAG = WS_OF + (size_t)2 * 1024 * 1024;
constexpr size_t WS_END = WS_FLAG + 4096;

constexpr float QSCALE = 0.25f * 1.4426950408889634f;

DI unsigned lds_addr(const void* p) { return (unsigned)(uintptr_t)(LDS_AS const char*)p; }
DI void glds16(const void* sbase, unsigned voff, unsigned ldst) {
  unsigned keep;
  asm volatile("s_mov_b32 %0, m0\n\ts_mov_b32 m0, %3\n\ts_nop 0\n\tglobal_load_lds_dwordx4 %1, %2\n\ts_mov_b32 m0, %0"
               : "=&s"(keep) : "v"(voff), "s"(sbase), "s"(ldst) : "memory");
}
#define WAIT_VM(n) asm volatile("s_waitcnt vmcnt(" #n ")" ::: "memory")
#define WAIT_LGKM0() asm volatile("s_waitcnt lgkmcnt(0)" ::: "memory")
#define BARRIER() __builtin_amdgcn_s_barrier()
template <int N> DI void wait_vm() { asm volatile("s_waitcnt vmcnt(%0)" ::"i"(N) : "memory"); }

DI __amdgpu_buffer_rsrc_t ws_rsrc(const void* ws) { return __builtin_amdgcn_make_buffer_rsrc((void*)ws, 0, (int)WS_END, 0x00020000); }
template <typename T> DI void st16(__amdgpu_buffer_rsrc_t rs, unsigned off, T v) {
  __builtin_amdgcn_raw_buffer_store_b128(__builtin_bit_cast(u32x4, v), rs, off, 0, 16);
}
DI bf16x8 lds_b128(const char* sm, unsigned off) { return *(const bf16x8*)(sm + off); }
DI bf16x4 lds_tr(const char* sm, unsigned off) {
  return __builtin_amdgcn_ds_read_tr16_b64_v4bf16((LDS_AS bf16x4*)(LDS_AS char*)(sm + off));
}
DI bf16x8 cat8(bf16x4 lo, bf16x4 hi) { return __builtin_shufflevector(lo, hi, 0, 1, 2, 3, 4, 5, 6, 7); }
DI bf16x8 pack_lo(const f32x16& v) { return __builtin_convertvector(__builtin_shufflevector(v, v, 0, 1, 2, 3, 4, 5, 6, 7), bf16x8); }
DI bf16x8 pack_hi(const f32x16& v) { return __builtin_convertvector(__builtin_shufflevector(v, v, 8, 9, 10, 11, 12, 13, 14, 15), bf16x8); }
DI float bf_lo(unsigned u) { return __uint_as_float(u << 16); }
DI float bf_hi(unsigned u) { return __uint_as_float(u & 0xffff0000u); }
DI int vslot(int d, int h) {
  int s = (d < 4) ? d : (d >= 12) ? d - 8 : (d < 11) ? d + 4 : -1;
  return (s >= 0) ? (16 * h + s) : (32 + h);
}

DI LDS_AS unsigned* lds_word(char* sm, int off) { return (LDS_AS unsigned*)(LDS_AS char*)(sm + off); }
DI void flag_add(char* sm, int off, int lane, unsigned n = 1u) {
  if (lane == 0) __hip_atomic_fetch_add(lds_word(sm, off), n, __ATOMIC_RELAXED, __HIP_MEMORY_SCOPE_WORKGROUP);
  asm volatile("" ::: "memory");
}
DI unsigned flag_peek(char* sm, int off) { return __hip_atomic_load(lds_word(sm, off), __ATOMIC_RELAXED, __HIP_MEMORY_SCOPE_WORKGROUP); }
template <int SLEEP = 1>
DI void flag_wait(char* sm, int off, unsigned target) {
  while ((unsigned)__builtin_amdgcn_readfirstlane(flag_peek(sm, off)) < target) __builtin_amdgcn_s_sleep(SLEEP);
  asm volatile("" ::: "memory");
}

constexpr int K1_SLOT = 28672, K1_WOFF = 16384, K1_NSLOT = 5;
constexpr int K1_STATS = K1_SLOT * K1_NSLOT;
constexpr int K1_WPART = K1_STATS + 1024;
constexpr int K1_FLG = K1_WPART + 768;
constexpr int K1_SIDE = K1_FLG + 256;
constexpr int K1_LDS = K1_SIDE + 16384;
#define K1_READY(s) (K1_FLG + 4 * (s))
#define K1_DONE(s) (K1_FLG + 32 + 4 * (s))
#define K1_FIN (K1_FLG + 44)

template <int NV>
DI void k1_gemm_wave(char* sm, int w, int lane, int rg, int cg, char* ws) {
  const int h = lane >> 5, r31 = lane & 31;
  unsigned aoff[4];
  {
    const int row = 32 * w + r31;
#pragma unroll
    for (int ks = 0; ks < 4; ++ks) aoff[ks] = row * 128 + (((2 * ks + h) ^ ((row >> 1) & 7)) << 4);
  }
  const unsigned boff = K1_WOFF + (8 * h + ((lane & 15) >> 2)) * 192 + (16 * ((lane >> 4) & 1) + 4 * (lane & 3)) * 2;
  f32x16 acc[3] = {};
  bf16x8 fa[4], fb[4][3];
#pragma unroll
  for (int i = 2; i < 4; ++i) {
    fa[i] = bf16x8{};
#pragma unroll
    for (int j = 0; j < 3; ++j) fb[i][j] = bf16x8{};
  }
#define K1_MMA(ps)                                                        \
  _Pragma("unroll") for (int j = 0; j < 3; ++j) {                         \
    if (j >= 3 - NV) acc[j] = MFMA(fa[ps], fb[ps][j], acc[j]);   \
    else acc[j] = MFMA(fb[ps][j], fa[ps], acc[j]);               \
  }
  int slot = 0;
  unsigned rdy = 0;
#pragma nounroll
  for (int s = 0; s < 8; ++s) {
    if ((unsigned)__builtin_amdgcn_readfirstlane(rdy) < 4u) flag_wait(sm, K1_READY(s), 4);
    asm volatile("" ::: "memory");
    const char* sl = sm + slot;
#pragma unroll
    for (int ks = 0; ks < 4; ++ks) {
      if (ks == 2) rdy = flag_peek(sm, K1_READY(s + 1));
      fa[ks] = lds_b128(sl, aoff[ks]);
#pragma unroll
      for (int j = 0; j < 3; ++j)
        fb[ks][j] = cat8(lds_tr(sl, boff + ks * 16 * 192 + 64 * j), lds_tr(sl, boff + (ks * 16 + 4) * 192 + 64 * j));
      __builtin_amdgcn_sched_barrier(0);
      K1_MMA((ks + 2) & 3);
      __builtin_amdgcn_sched_barrier(0);
      if (ks == 1 && s >= 1 && s <= 3) flag_add(sm, K1_DONE(s - 1), lane);
    }
    slot += K1_SLOT;
    if (slot == K1_NSLOT * K1_SLOT) slot = 0;
  }
  K1_MMA(2);
  K1_MMA(3);
  asm volatile("" : "+v"(acc[0]), "+v"(acc[1]), "+v"(acc[2]));
  flag_wait(sm, K1_FIN, 4);
  const int rt = rg * 4 + w;
  const __amdgpu_buffer_rsrc_t rs = ws_rsrc(ws);
  const f32x2* stats = (const f32x2*)(sm + K1_STATS) + 32 * w;
  const float* wsum = (const float*)(sm + K1_WPART);
#pragma unroll
  for (int j = 0; j < 3; ++j) {
    const int ct = cg * 3 + j, role = ct >> 4, hp = (ct & 15) * 2;
    f32x16 v;
    if (j >= 3 - NV) {
      const float wsn = wsum[32 * j + r31] + wsum[96 + 32 * j + r31];
#pragma unroll
      for (int g = 0; g < 4; ++g) {
        f32x4 s0 = *(const f32x4*)(stats + 8 * g + 4 * h), s1 = *(const f32x4*)(stats + 8 * g + 4 * h + 2);
        v[4 * g + 0] = acc[j][4 * g + 0] * s0[0] + s0[1] * wsn;
        v[4 * g + 1] = acc[j][4 * g + 1] * s0[2] + s0[3] * wsn;
        v[4 * g + 2] = acc[j][4 * g + 2] * s1[0] + s1[1] * wsn;
        v[4 * g + 3] = acc[j][4 * g + 3] * s1[2] + s1[3] * wsn;
      }
      const unsigned base = (unsigned)WS_KV + (hp + (r31 >> 4)) * KVHEAD + rt * KVTILE + 1024;
      const int sl = vslot(r31 & 15, h);
      st16(rs, base + sl * 16, pack_lo(v));
      st16(rs, base + VBLK + sl * 16, pack_hi(v));
      if ((lane & 15) == 0)
        st16(rs, base + h * VBLK + 15 * 16, u32x4{0x3f803f80u, 0x3f803f80u, 0x3f803f80u, 0x3f803f80u});
    } else {
      f32x2 st = stats[r31];
      if (role == 0) { st[0] *= QSCALE; st[1] *= QSCALE; }
#pragma unroll
      for (int g = 0; g < 4; ++g) {
        f32x4 wsv = *(const f32x4*)(wsum + 32 * j + 8 * g + 4 * h) + *(const f32x4*)(wsum + 96 + 32 * j + 8 * g + 4 * h);
#pragma unroll
        for (int e = 0; e < 4; ++e) v[4 * g + e] = acc[j][4 * g + e] * st[0] + st[1] * wsv[e];
      }
      if (role == 0) {
        const unsigned base = (unsigned)WS_QF + (hp * 64 + rt) * 1024 + lane * 16;
        st16(rs, base, pack_lo(v));
        st16(rs, base + 64 * 1024, pack_hi(v));
      } else {
        const unsigned base = (unsigned)WS_KV + hp * KVHEAD + rt * KVTILE + lane * 16;
        st16(rs, base, pack_lo(v));
        st16(rs, base + KVHEAD, pack_hi(v));
      }
    }
  }
}


#define MFMA16(a, b, c) __builtin_amdgcn_mfma_f32_16x16x32_bf16((a), (b), (c), 0, 0, 0)
DI void k1_stats_wave(char* sm, const char* __restrict__ xg, int hw, int lane, int rg, int cg, char* ws) {
  const int r15 = lane & 15, kq = lane >> 4;
  unsigned aoff[4];
#pragma unroll
  for (int i = 0; i < 4; ++i) {
    const int row = 32 * hw + 16 * (i >> 1) + r15, c = 4 * (i & 1) + kq;
    aoff[i] = row * 128 + ((c ^ ((row >> 1) & 7)) << 4);
  }
  const unsigned boff = K1_WOFF + (32 * (hw & 1) + 8 * kq + (r15 >> 2)) * 192 + (48 * (hw >> 1) + 4 * (lane & 3)) * 2;
  const bf16x8 ones = __builtin_bit_cast(bf16x8, u32x4{0x3f803f80u, 0x3f803f80u, 0x3f803f80u, 0x3f803f80u});
  f32x4 gram[2] = {}, rsum[2] = {}, wacc[3] = {};
  bf16x8 xa[2][4] = {}, wb[2][3] = {};
  int slot = 0;
  u32x4 xr2[2];
#pragma unroll
  for (int i = 0; i < 2; ++i)
    xr2[i] = *(const u32x4*)(xg + (size_t)(rg * 128 + cg * 8 + (lane & 7)) * 1024 + ((lane >> 3) + 8 * (2 * hw + i)) * 16);
  auto math = [&](const bf16x8 (&xp)[4], const bf16x8 (&wp)[3]) {
#pragma unroll
    for (int i = 0; i < 4; ++i) {
      gram[i >> 1] = MFMA16(xp[i], xp[i], gram[i >> 1]);
      rsum[i >> 1] = MFMA16(xp[i], ones, rsum[i >> 1]);
    }
#pragma unroll
    for (int j = 0; j < 3; ++j) wacc[j] = MFMA16(ones, wp[j], wacc[j]);
  };
#pragma nounroll
  for (int s2 = 0; s2 < 4; ++s2) {
#pragma unroll
    for (int par = 0; par < 2; ++par) {
      flag_wait(sm, K1_READY(2 * s2 + par), 4);
      const char* sl = sm + slot;
#pragma unroll
      for (int i = 0; i < 4; ++i) xa[par][i] = lds_b128(sl, aoff[i]);
#pragma unroll
      for (int j = 0; j < 3; ++j) wb[par][j] = cat8(lds_tr(sl, boff + 32 * j), lds_tr(sl, boff + 4 * 192 + 32 * j));
      __builtin_amdgcn_sched_barrier(0);
      math(xa[par ^ 1], wb[par ^ 1]);
      __builtin_amdgcn_sched_barrier(0);
      WAIT_LGKM0();
      if (2 * s2 + par <= 2) flag_add(sm, K1_DONE(2 * s2 + par), lane);
      slot += K1_SLOT;
      if (slot == K1_NSLOT * K1_SLOT) slot = 0;
    }
  }
  math(xa[1], wb[1]);
  if (lane < 16) {
#pragma unroll
    for (int j = 0; j < 3; ++j) *(float*)(sm + K1_WPART + ((hw & 1) * 96 + 48 * (hw >> 1) + 16 * j + lane) * 4) = wacc[j][0];
  }
  if ((r15 >> 2) == kq) {
#pragma unroll
    for (int g2 = 0; g2 < 2; ++g2) {
      const int e = r15 & 3;
      const float sq = e == 0 ? gram[g2][0] : e == 1 ? gram[g2][1] : e == 2 ? gram[g2][2] : gram[g2][3];
      const float sum = e == 0 ? rsum[g2][0] : e == 1 ? rsum[g2][1] : e == 2 ? rsum[g2][2] : rsum[g2][3];
      const int row = 32 * hw + 16 * g2 + r15;
      const float mean = sum * (1.f / 512.f);
      float var = sq * (1.f / 512.f) - mean * mean;
      if (__builtin_expect(!(var > 1e-3f * mean * mean), 0)) {
        const unsigned* xr = (const unsigned*)(xg + (size_t)(rg * 128 + row) * 1024);
        float acc = 0.f;
        for (int i = 0; i < 256; ++i) {
          const unsigned u = xr[i];
          const float a = bf_lo(u) - mean, b = bf_hi(u) - mean;
          acc = fmaf(a, a, fmaf(b, b, acc));
        }
        var = acc * (1.f / 512.f);
      }
      const float rstd = __builtin_amdgcn_rsqf(var + 1e-5f);
      *(f32x2*)(sm + K1_STATS + row * 8) = f32x2{rstd, -mean * rstd};
    }
  }
  WAIT_LGKM0();
  flag_add(sm, K1_FIN, lane);
  flag_wait(sm, K1_FIN, 4);
  {
    const int p = lane & 7, lrow = cg * 8 + p;
    const f32x2 st = *(const f32x2*)(sm + K1_STATS + lrow * 8);
    const int grow = rg * 128 + lrow;
#pragma unroll
    for (int i = 0; i < 2; ++i) {
      const int c = (lane >> 3) + 8 * (2 * hw + i);
      const u32x4 u = xr2[i];
      f32x8 v;
#pragma unroll
      for (int e = 0; e < 4; ++e) { v[2 * e] = bf_lo(u[e]) * st[0] + st[1]; v[2 * e + 1] = bf_hi(u[e]) * st[0] + st[1]; }
      st16(ws_rsrc(ws), (unsigned)WS_XN + (((grow >> 5) * 32 + (c >> 1)) * 64 + (c & 1) * 32 + (grow & 31)) * 16,
           __builtin_convertvector(v, bf16x8));
    }
  }
}

DI void k1_loader_wave(char* sm, unsigned sbase, const char* xg, const char* wqkv, const char* w1, const char* w2,
                       const char* wo, int lw, int lane, int bid, int rg, int cg, int rot, char* ws) {
  unsigned xoff[2], woff[3];
#pragma unroll
  for (int p = 0; p < 2; ++p) {
    int c = (lane & 7) ^ ((4 * p + (lane >> 4)) & 7);
    xoff[p] = (unsigned)((rg * 128 + (lane >> 3)) * 1024 + c * 16);
  }
#pragma unroll
  for (int i = 0; i < 3; ++i) {
    int idx = 64 * i + lane;
    int row = idx / 12, ch = idx - row * 12;
    woff[i] = (unsigned)(row * 3072 + cg * 192 + ch * 16);
  }
  auto issue = [&](int s, int slot, int g) {
    const int ks = (s + rot) & 7;
    const unsigned dst = sbase + slot;
#pragma unroll
    for (int i = 0; i < 3; ++i)
      glds16(wqkv + (size_t)(ks * 64 + g * 16) * 3072, woff[i], dst + K1_WOFF + (3 * g + i) * 1024);
#pragma unroll
    for (int i = 0; i < 4; ++i) glds16(xg + ks * 128 + (4 * g + i) * 8192, xoff[i & 1], dst + (4 * g + i) * 1024);
  };
  issue(0, 0, lw);
  issue(1, K1_SLOT, lw);
  const char* swsrc; int sld, kb, nb, sks; size_t sdst; bool perm;
  if (bid < 112) { swsrc = w1; sld = FF_ * 2; kb = bid / 14; nb = bid - kb * 14; sks = 32; sdst = WS_W1; perm = false; }
  else if (bid < 224) { int v = bid - 112; swsrc = w2; sld = D_ * 2; kb = v >> 2; nb = v & 3; sks = 112; sdst = WS_W2; perm = true; }
  else { int v = bid - 224; swsrc = wo; sld = D_ * 2; kb = v >> 2; nb = v & 3; sks = 32; sdst = WS_WO; perm = true; }
  const int h = lane >> 5, q = (lane & 15) >> 2;
  const unsigned cb = (16 * ((lane >> 4) & 1) + 4 * (lane & 3)) * 2;
  if (lw < 3) {
    const int sl = (2 + lw) * K1_SLOT;
    issue(2 + lw, sl, 0);
    wait_vm<14>(); flag_add(sm, K1_READY(0), lane);
    issue(2 + lw, sl, 1);
    wait_vm<14>(); flag_add(sm, K1_READY(1), lane);
    issue(2 + lw, sl, 2);
    issue(2 + lw, sl, 3);
  } else {
    wait_vm<7>(); flag_add(sm, K1_READY(0), lane);
    wait_vm<0>(); flag_add(sm, K1_READY(1), lane);
    flag_wait(sm, K1_DONE(0), 8);
#pragma unroll
    for (int g = 0; g < 4; ++g) issue(5, 0, g);
  }
  wait_vm<0>(); flag_add(sm, K1_READY(2 + lw), lane, 4u);
  flag_wait(sm, K1_DONE(1), 8);
  issue(6, K1_SLOT, lw);
  flag_wait(sm, K1_DONE(2), 8);
  issue(7, 2 * K1_SLOT, lw);
  wait_vm<7>(); flag_add(sm, K1_READY(6), lane);
#pragma unroll
  for (int i = 0; i < 4; ++i) {
    const int p = 4 * lw + i;
    glds16(swsrc + (size_t)(kb * 64 + 4 * p) * sld + nb * 256, (unsigned)((lane >> 4) * sld + (lane & 15) * 16),
           sbase + K1_SIDE + p * 1024);
  }
  wait_vm<4>(); flag_add(sm, K1_READY(7), lane);
  WAIT_VM(0);
  {
    const int r0 = perm ? 4 * h : 8 * h, r1 = perm ? 8 + 4 * h : 8 * h + 4;
    const unsigned o0 = K1_SIDE + (16 * lw + r0 + q) * 256 + cb, o1 = K1_SIDE + (16 * lw + r1 + q) * 256 + cb;
#pragma unroll
    for (int nt = 0; nt < 4; ++nt) {
      bf16x8 f = cat8(lds_tr(sm, o0 + nt * 64), lds_tr(sm, o1 + nt * 64));
      st16(ws_rsrc(ws), (unsigned)sdst + ((nb * 4 + nt) * sks + kb * 4 + lw) * 1024 + lane * 16, f);
    }
  }
}

extern "C" __global__ __launch_bounds__(768) void k1_qkv(const char* __restrict__ x, const char* __restrict__ wqkv,
                                              const char* __restrict__ w1, const char* __restrict__ w2,
                                              const char* __restrict__ wo, char* __restrict__ ws) {
  extern __shared__ __attribute__((aligned(16))) char sm[];
  const int lane = threadIdx.x & 63, wave = __builtin_amdgcn_readfirstlane(threadIdx.x >> 6);
  const int bid = blockIdx.x, xcd = bid & 7, idx = bid >> 3;
  const int rg = 4 * (xcd >> 1) + (idx & 3), cg = 8 * (xcd & 1) + (idx >> 2);
  if (threadIdx.x >= 256 && threadIdx.x < 272) *(unsigned*)(sm + K1_FLG + (threadIdx.x - 256) * 4) = 0u;
  WAIT_LGKM0();
  BARRIER();
  if (wave < 4) {
    const int rot = ((idx >> 2) + 2 * (idx & 3)) & 7;
    k1_loader_wave(sm, lds_addr(sm), x, wqkv, w1, w2, wo, wave, lane, bid, rg, cg, rot, ws);
  } else if (wave < 8) {
    if (cg < 10) k1_gemm_wave<0>(sm, wave - 4, lane, rg, cg, ws);
    else if (cg == 10) k1_gemm_wave<1>(sm, wave - 4, lane, rg, cg, ws);
    else k1_gemm_wave<3>(sm, wave - 4, lane, rg, cg, ws);
  } else {
    if (bid == 0) st16(ws_rsrc(ws), (unsigned)WS_FLAG + (wave - 8) * 1024 + lane * 16, u32x4{0u, 0u, 0u, 0u});
#pragma unroll
    for (int i = 0; i < 2; ++i)
      st16(ws_rsrc(ws), (unsigned)WS_OF + bid * 8192 + (wave - 8) * 2048 + i * 1024 + lane * 16, u32x4{~0u, ~0u, ~0u, ~0u});
    k1_stats_wave(sm, x, wave - 8, lane, rg, cg, ws);
  }
}

constexpr int K2_FLG = KVHEAD;
constexpr int K2_LDS = K2_FLG + 64;
constexpr float SM_DELTA = 80.f;
typedef __attribute__((address_space(1))) unsigned gu32;
#define SB() __builtin_amdgcn_sched_barrier(0)

DI float xhalf_max(float v) {
  auto r = __builtin_amdgcn_permlane32_swap(__float_as_uint(v), __float_as_uint(v), false, false);
  return fmaxf(__uint_as_float(r[0]), __uint_as_float(r[1]));
}
DI float max16(const f32x16& s) {
  float m01 = fmaxf(fmaxf(s[0], s[1]), s[2]), m23 = fmaxf(fmaxf(s[3], s[4]), s[5]);
  float m45 = fmaxf(fmaxf(s[6], s[7]), s[8]), m67 = fmaxf(fmaxf(s[9], s[10]), s[11]);
  float m89 = fmaxf(fmaxf(s[12], s[13]), s[14]);
  return fmaxf(fmaxf(fmaxf(m01, m23), m45), fmaxf(fmaxf(m67, m89), s[15]));
}
DI void lds_signal(char* sm, int word, int lane) { flag_add(sm, K2_FLG + 4 * word, lane); }
DI void lds_wait(char* sm, int word, unsigned target) { flag_wait(sm, K2_FLG + 4 * word, target); }

template <int NKS, int PF, int AUXB>
DI void dgemm64_ring(__amdgpu_buffer_rsrc_t rs, unsigned a0, unsigned a1, unsigned b0, unsigned b1, int rot, f32x16 (&acc)[4]) {
  u32x4 A0[PF], A1[PF], B0[PF], B1[PF];
  int ko = rot * 1024;
#pragma unroll
  for (int kk = 0; kk < NKS + PF - 1; ++kk) {
    if (kk < NKS) {
      const int sl = kk % PF;
      A0[sl] = __builtin_amdgcn_raw_buffer_load_b128(rs, a0, ko, 0);
      B0[sl] = __builtin_amdgcn_raw_buffer_load_b128(rs, b0, ko, AUXB);
      A1[sl] = __builtin_amdgcn_raw_buffer_load_b128(rs, a1, ko, 0);
      B1[sl] = __builtin_amdgcn_raw_buffer_load_b128(rs, b1, ko, AUXB);
      ko += 1024;
      if (ko == NKS * 1024) ko = 0;
    }
    SB();
    if (kk >= PF - 1) {
      const int sl = (kk - (PF - 1)) % PF;
      const bf16x8 fa0 = __builtin_bit_cast(bf16x8, A0[sl]), fa1 = __builtin_bit_cast(bf16x8, A1[sl]);
      const bf16x8 fb0 = __builtin_bit_cast(bf16x8, B0[sl]), fb1 = __builtin_bit_cast(bf16x8, B1[sl]);
      acc[0] = MFMA(fa0, fb0, acc[0]);
      acc[2] = MFMA(fa1, fb0, acc[2]);
      acc[1] = MFMA(fa0, fb1, acc[1]);
      acc[3] = MFMA(fa1, fb1, acc[3]);
    }
    SB();
  }
  asm volatile("" : "+v"(acc[0]), "+v"(acc[1]), "+v"(acc[2]), "+v"(acc[3]));
}

DI void k2_ffn_wave(char* sm, const char* __restrict__ x, char* __restrict__ ws, char* __restrict__ out, int w, int lane, int panel, int ci, int head) {
  {
    const unsigned sb = lds_addr(sm) + w * 1024, dl = lane * 16;
    const char* kvg = ws + WS_KV + (size_t)head * KVHEAD + w * 1024;
#pragma unroll
    for (int i = 0; i < 8; ++i) glds16(kvg + i * 4096, dl, sb + i * 4096);
    WAIT_LGKM0();
    BARRIER();
    WAIT_VM(4);
    lds_signal(sm, 0, lane);
#pragma unroll
    for (int i = 8; i < 16; ++i) glds16(kvg + i * 4096, dl, sb + i * 4096);
    WAIT_VM(4);
    lds_signal(sm, 1, lane);
#pragma unroll
    for (int i = 16; i < 34; ++i) glds16(kvg + i * 4096, dl, sb + i * 4096);
    WAIT_VM(0);
    lds_signal(sm, 2, lane);
  }
  __builtin_amdgcn_s_setprio(2);
  const __amdgpu_buffer_rsrc_t rs = __builtin_amdgcn_make_buffer_rsrc((void*)ws, 0, (int)WS_END, 0x00020000);
  gu32* flags = (gu32*)(ws + WS_FLAG) + panel * 128;
  if (ci < 28) {
    const int rt0 = panel * 8 + 2 * w, nt0 = 2 * ci;
    const unsigned a0 = (unsigned)WS_W1 + nt0 * 32768 + lane * 16, b0 = (unsigned)WS_XN + rt0 * 32768 + lane * 16;
    f32x16 acc[4] = {};
    dgemm64_ring<32, 5, 0>(rs, a0, a0 + 32768, b0, b0 + 32768, ci, acc);
#pragma unroll
    for (int i = 0; i < 2; ++i)
#pragma unroll
      for (int j = 0; j < 2; ++j) {
        f32x16 v = acc[2 * i + j];
#pragma unroll
        for (int e = 0; e < 16; ++e) v[e] = fmaxf(v[e], 0.f);
        const unsigned dst = (unsigned)WS_HF + ((rt0 + j) * 112 + (nt0 + i) * 2) * 1024 + lane * 16;
        __builtin_amdgcn_raw_buffer_store_b128(__builtin_bit_cast(u32x4, pack_lo(v)), rs, dst, 0, 16);
        __builtin_amdgcn_raw_buffer_store_b128(__builtin_bit_cast(u32x4, pack_hi(v)), rs, dst + 1024, 0, 16);
      }
    WAIT_VM(0);
    if (lane == 0) __hip_atomic_store(flags + w * 32 + ci, 1u, __ATOMIC_RELAXED, __HIP_MEMORY_SCOPE_AGENT);
  }
  const int rp2 = ci & 3, np2 = ci >> 2;
  {
    __builtin_amdgcn_s_setprio(0);
    gu32* f = flags + rp2 * 32 + 7 * w + min(lane, 6);
    for (unsigned spins = 0; spins < (1u << 20); ++spins) {
      unsigned v = __hip_atomic_load(f, __ATOMIC_RELAXED, __HIP_MEMORY_SCOPE_AGENT);
      if (__all(v == 1u)) break;
      __builtin_amdgcn_s_sleep(8);
    }
    __builtin_amdgcn_fence(__ATOMIC_ACQUIRE, "wavefront");
    asm volatile("" ::: "memory");
  }
  __builtin_amdgcn_s_setprio(2);
  const int rt0 = panel * 8 + 2 * rp2, nt0 = 2 * np2;
  f32x16 acc[4] = {};
  {
    const unsigned a0 = (unsigned)WS_W2 + (nt0 * 112 + 28 * w) * 1024 + lane * 16;
    const unsigned b0 = (unsigned)WS_HF + (rt0 * 112 + 28 * w) * 1024 + lane * 16;
    dgemm64_ring<28, 5, 16>(rs, a0, a0 + 112 * 1024, b0, b0 + 112 * 1024, (7 * rp2 + 3 * np2) % 28, acc);
  }
  const int h = lane >> 5, r31 = lane & 31;
  const unsigned eoff = (unsigned)(((rt0 + (w & 1)) * 32 + r31) * D_ + (nt0 + (w >> 1)) * 32 + 16 * h) * 2;
  u32x4 xv[2];
#pragma unroll
  for (int c = 0; c < 2; ++c) xv[c] = *(const u32x4*)(x + eoff + 16 * c);
  __builtin_amdgcn_s_setprio(0);
  flag_wait<6>(sm, K2_FLG + 12, 8);
  {
    const int wl = 65536 + w * 16384;
    const char* wog = ws + WS_WO + (size_t)(nt0 * 32 + 8 * w) * 1024;
#pragma unroll
    for (int i = 0; i < 16; ++i) glds16(wog + (i >> 3) * 32768 + (i & 7) * 1024, lane * 16, lds_addr(sm) + wl + i * 1024);
    gu32* f = (gu32*)(ws + WS_OF + ((rt0 + ((lane >> 3) & 1)) * 32 + 8 * w + (lane & 7)) * 1024 + 1020);
    for (unsigned spins = 0; spins < (1u << 20); ++spins) {
      unsigned v = __hip_atomic_load(f, __ATOMIC_RELAXED, __HIP_MEMORY_SCOPE_AGENT);
      if (__all(v != ~0u)) break;
      __builtin_amdgcn_s_sleep(2);
    }
    asm volatile("" ::: "memory");
    const unsigned b0 = (unsigned)WS_OF + (rt0 * 32 + 8 * w) * 1024 + lane * 16;
    u32x4 B[16];
    for (unsigned spins = 0; spins < (1u << 16); ++spins) {
      asm volatile("" ::: "memory");
#pragma unroll
      for (int i = 0; i < 16; ++i) B[i] = __builtin_amdgcn_raw_buffer_load_b128(rs, b0, (i & 1) * 32768 + (i >> 1) * 1024, 16);
      unsigned m = 0;
#pragma unroll
      for (int i = 0; i < 16; ++i) m = max(max(m, B[i][0]), max(B[i][1], max(B[i][2], B[i][3])));
      if (__builtin_expect(!__any(m == ~0u), 1)) break;
    }
    WAIT_VM(0);
#pragma unroll
    for (int ks = 0; ks < 8; ++ks) {
      const bf16x8 fa0 = lds_b128(sm, wl + ks * 1024 + lane * 16), fa1 = lds_b128(sm, wl + 8192 + ks * 1024 + lane * 16);
      const bf16x8 fb0 = __builtin_bit_cast(bf16x8, B[2 * ks]), fb1 = __builtin_bit_cast(bf16x8, B[2 * ks + 1]);
      acc[0] = MFMA(fa0, fb0, acc[0]);
      acc[2] = MFMA(fa1, fb0, acc[2]);
      acc[1] = MFMA(fa0, fb1, acc[1]);
      acc[3] = MFMA(fa1, fb1, acc[3]);
    }
  }
  {
    f32x4* red = (f32x4*)sm + lane;
#pragma unroll
    for (int t = 0; t < 4; ++t)
#pragma unroll
      for (int g = 0; g < 4; ++g)
        red[((t * 4 + w) * 4 + g) * 64] = f32x4{acc[t][4 * g], acc[t][4 * g + 1], acc[t][4 * g + 2], acc[t][4 * g + 3]};
    WAIT_LGKM0();
    lds_signal(sm, 4, lane);
    lds_wait(sm, 4, 4);
    const f32x4* rd = (const f32x4*)sm + r31;
    const __amdgpu_buffer_rsrc_t ro = __builtin_amdgcn_make_buffer_rsrc((void*)out, 0, S_ * D_ * 2, 0x00020000);
#pragma unroll
    for (int c = 0; c < 2; ++c) {
      const int g = 2 * h + c;
      f32x4 lo = rd[((w * 4 + 0) * 4 + g) * 64], hi = rd[((w * 4 + 0) * 4 + g) * 64 + 32];
#pragma unroll
      for (int src = 1; src < 4; ++src) {
        lo += rd[((w * 4 + src) * 4 + g) * 64];
        hi += rd[((w * 4 + src) * 4 + g) * 64 + 32];
      }
      f32x8 v;
      v[0] = lo[0] + bf_lo(xv[c][0]); v[1] = lo[1] + bf_hi(xv[c][0]); v[2] = lo[2] + bf_lo(xv[c][1]); v[3] = lo[3] + bf_hi(xv[c][1]);
      v[4] = hi[0] + bf_lo(xv[c][2]); v[5] = hi[1] + bf_hi(xv[c][2]); v[6] = hi[2] + bf_lo(xv[c][3]); v[7] = hi[3] + bf_hi(xv[c][3]);
      __builtin_amdgcn_raw_buffer_store_b128(__builtin_bit_cast(u32x4, __builtin_convertvector(v, bf16x8)), ro, eoff + 16 * c, 0, 0);
    }
  }
}

extern "C" __global__ __launch_bounds__(768) void k2_attn(const char* __restrict__ x, char* __restrict__ ws, char* __restrict__ out) {
  extern __shared__ __attribute__((aligned(16))) char sm[];
  const int lane = threadIdx.x & 63, wave = __builtin_amdgcn_readfirstlane(threadIdx.x >> 6);
  const int bid = blockIdx.x, xcd = bid & 7, idx = bid >> 3;
  if (threadIdx.x < 16) *(unsigned*)(sm + K2_FLG + threadIdx.x * 4) = 0u;
  const int head = xcd * 4 + (idx >> 3), qb = idx & 7, rt = qb * 8 + wave;
  if (wave >= 8) {
    k2_ffn_wave(sm, x, ws, out, wave - 8, lane, xcd, idx, head);
    return;
  }
  const bf16x8 qf = *((const bf16x8*)(ws + WS_QF) + ((size_t)(head * 64 + rt) * 64 + lane));
  const int h = lane >> 5, r31 = lane & 31;
  const unsigned koff = lane * 16;
  const unsigned voff = 1024 + ((r31 < 16) ? vslot(r31, h) : 15) * 16;
  WAIT_LGKM0();
  BARRIER();
  f32x16 negm = {}, O = {};
#define EXP(v, i) v[i] = __builtin_amdgcn_exp2f(v[i])
#define TILE_STEP(SC, SN, OFFK2, OFFV0N, OFFV1)                                   \
  {                                                                               \
    SN = MFMA(kn, qf, negm);                                                      \
    SB();                                                                         \
    kn = lds_b128(kp, OFFK2);                                                     \
    EXP(SC, 0); EXP(SC, 1); EXP(SC, 2); EXP(SC, 3); EXP(SC, 4); EXP(SC, 5);       \
    SB();                                                                         \
    O = MFMA(v1p, p1p, O);                                                        \
    SB();                                                                         \
    v1p = lds_b128(vp, OFFV1);                                                    \
    EXP(SC, 6); EXP(SC, 7);                                                       \
    bf16x8 p0 = pack_lo(SC);                                                      \
    SB();                                                                         \
    EXP(SC, 8); EXP(SC, 9);                                                       \
    SB();                                                                         \
    O = MFMA(v0, p0, O);                                                          \
    SB();                                                                         \
    v0 = lds_b128(vp, OFFV0N);                                                    \
    EXP(SC, 10); EXP(SC, 11); EXP(SC, 12); EXP(SC, 13); EXP(SC, 14); EXP(SC, 15); \
    p1p = pack_hi(SC);                                                            \
    SB();                                                                         \
  }
#define TILE4()                                                        \
  TILE_STEP(sA, sB, 2 * KVTILE, 1 * KVTILE, 0 * KVTILE + VBLK);        \
  TILE_STEP(sB, sA, 3 * KVTILE, 2 * KVTILE, 1 * KVTILE + VBLK);        \
  TILE_STEP(sA, sB, 4 * KVTILE, 3 * KVTILE, 2 * KVTILE + VBLK);        \
  TILE_STEP(sB, sA, 5 * KVTILE, 4 * KVTILE, 3 * KVTILE + VBLK);        \
  kp += 4 * KVTILE; vp += 4 * KVTILE;
  {
    const char* kp = sm + koff;
    const char* vp = sm + voff;
    lds_wait(sm, 0, 4);
    const bf16x8 k0 = lds_b128(kp, 0);
    bf16x8 kn = lds_b128(kp, KVTILE), v0 = lds_b128(vp, 0), v1p = lds_b128(vp, VBLK), p1p = {};
    {
      const f32x16 z = {};
      f32x16 s0 = MFMA(k0, qf, z), s1 = MFMA(kn, qf, z);
      const float nm = -(xhalf_max(fmaxf(max16(s0), max16(s1))) + SM_DELTA);
#pragma unroll
      for (int e = 0; e < 16; ++e) negm[e] = nm;
    }
    f32x16 sA = MFMA(k0, qf, negm), sB;
    int ev = 4;
    for (int t = 0; t < 64; t += 4) {
      if (__builtin_expect(t == ev, 0)) {
        lds_wait(sm, t == 4 ? 1 : 2, 4);
        if (t != 4 && wave >= 4) __builtin_amdgcn_s_setprio(1);
        ev = (t == 4) ? 20 : -100;
      }
      TILE4();
      asm volatile("" : "+v"(O), "+v"(sA), "+v"(p1p));
    }
    O = MFMA(v1p, p1p, O);
  }
  float l = O[8];
  bool ok = (l < 0x1p120f);
  float omax = 0.f;
#pragma unroll
  for (int e = 0; e < 8; ++e) { ok = ok && (fabsf(O[e]) < 0x1p126f); omax = fmaxf(omax, fabsf(O[e])); }
  ok = ok && (omax > 0x1p-100f);
  if (!__all(ok)) {
    float m = -INFINITY;
#pragma unroll
    for (int e = 0; e < 16; ++e) O[e] = 0.f;
    for (int t = 0; t < 64; ++t) {
      f32x16 z = {};
      f32x16 sc = MFMA(lds_b128(sm, koff + t * KVTILE), qf, z);
      bf16x8 v0 = lds_b128(sm, voff + t * KVTILE), v1 = lds_b128(sm, voff + t * KVTILE + VBLK);
      float mn = fmaxf(m, xhalf_max(max16(sc)));
      float alpha = __builtin_amdgcn_exp2f(m - mn);
      m = mn;
#pragma unroll
      for (int e = 0; e < 16; ++e) { O[e] *= alpha; sc[e] = __builtin_amdgcn_exp2f(sc[e] - mn); }
      O = MFMA(v0, pack_lo(sc), O);
      O = MFMA(v1, pack_hi(sc), O);
    }
    l = O[8];
  }
  const float rl = __builtin_amdgcn_rcpf(l);
#pragma unroll
  for (int e = 0; e < 8; ++e) O[e] *= rl;
  u32x4 po = __builtin_bit_cast(u32x4, pack_lo(O));
#pragma unroll
  for (int e = 0; e < 4; ++e) po[e] = min(po[e], 0xFFFFFFFEu);
  __builtin_amdgcn_raw_buffer_store_b128(po, ws_rsrc(ws), (unsigned)WS_OF + (rt * 32 + head) * 1024 + lane * 16, 0, 16);
  WAIT_LGKM0();
  lds_signal(sm, 3, lane);
}

extern "C" void kernel_launch(void* const* d_in, const int* in_sizes, int n_in, void* d_out, int out_size, void* d_ws,
                              size_t ws_size, hipStream_t stream) {
  const char* x = (const char*)d_in[0];
  const char* wqkv = (const char*)d_in[1];
  const char* wo = (const char*)d_in[2];
  const char* w1 = (const char*)d_in[4];
  const char* w2 = (const char*)d_in[5];
  char* ws = (char*)d_ws;
  (void)hipFuncSetAttribute((const void*)k1_qkv, hipFuncAttributeMaxDynamicSharedMemorySize, K1_LDS);
  (void)hipFuncSetAttribute((const void*)k2_attn, hipFuncAttributeMaxDynamicSharedMemorySize, K2_LDS);
  hipLaunchKernelGGL(k1_qkv, dim3(256), dim3(768), K1_LDS, stream, x, wqkv, w1, w2, wo, ws);
  hipLaunchKernelGGL(k2_attn, dim3(256), dim3(768), K2_LDS, stream, x, ws, (char*)d_out);
}
```

```cpp
#include <hip/hip_runtime.h>
#include <hip/hip_bf16.h>
#include <stdint.h>

typedef __attribute__((ext_vector_type(8))) __bf16 bf16x8;
typedef __attribute__((ext_vector_type(4))) __bf16 bf16x4;
typedef __attribute__((ext_vector_type(16))) float f32x16;
typedef __attribute__((ext_vector_type(8))) float f32x8;
typedef __attribute__((ext_vector_type(4))) float f32x4;
typedef __attribute__((ext_vector_type(2))) float f32x2;
typedef __attribute__((ext_vector_type(4))) unsigned u32x4;
typedef __attribute__((ext_vector_type(2))) unsigned u32x2;
#define LDS_AS __attribute__((address_space(3)))
#define DI __device__ __forceinline__
#define MFMA(a, b, c) __builtin_amdgcn_mfma_f32_32x32x16_bf16((a), (b), (c), 0, 0, 0)

constexpr int S_ = 2048, D_ = 512, FF_ = 1792, NH_ = 32;
constexpr int KVTILE = 2112;
constexpr int VBLK = 544;
constexpr int KVHEAD = 136 * 1024;
constexpr size_t WS_QF = 0;
constexpr size_t WS_KV = WS_QF + (size_t)2 * 1024 * 1024;
constexpr size_t WS_XN = WS_KV + (size_t)NH_ * KVHEAD;
constexpr size_t WS_W1 = WS_XN + (size_t)2 * 1024 * 1024;
constexpr size_t WS_W2 = WS_W1 + (size_t)1792 * 1024;
constexpr size_t WS_WO = WS_W2 + (size_t)1792 * 1024;
constexpr size_t WS_HF = WS_WO + (size_t)512 * 1024;
constexpr size_t WS_OF = WS_HF + (size_t)64 * 112 * 1024;
constexpr size_t WS_FLAG = WS_OF + (size_t)2 * 1024 * 1024;
constexpr size_t WS_END = WS_FLAG + 4096;

constexpr float QSCALE = 0.25f * 1.4426950408889634f;

DI unsigned lds_addr(const void* p) { return (unsigned)(uintptr_t)(LDS_AS const char*)p; }
DI void glds16(const void* sbase, unsigned voff, unsigned ldst) {
  unsigned keep;
  asm volatile("s_mov_b32 %0, m0\n\ts_mov_b32 m0, %3\n\ts_nop 0\n\tglobal_load_lds_dwordx4 %1, %2\n\ts_mov_b32 m0, %0"
               : "=&s"(keep) : "v"(voff), "s"(sbase), "s"(ldst) : "memory");
}
#define WAIT_VM(n) asm volatile("s_waitcnt vmcnt(" #n ")" ::: "memory")
#define WAIT_LGKM0() asm volatile("s_waitcnt lgkmcnt(0)" ::: "memory")
#define BARRIER() __builtin_amdgcn_s_barrier()
template <int N> DI void wait_vm() { asm volatile("s_waitcnt vmcnt(%0)" ::"i"(N) : "memory"); }

DI __amdgpu_buffer_rsrc_t ws_rsrc(const void* ws) { return __builtin_amdgcn_make_buffer_rsrc((void*)ws, 0, (int)WS_END, 0x00020000); }
template <typename T> DI void st16(__amdgpu_buffer_rsrc_t rs, unsigned off, T v) {
  __builtin_amdgcn_raw_buffer_store_b128(__builtin_bit_cast(u32x4, v), rs, off, 0, 16);
}
DI bf16x8 lds_b128(const char* sm, unsigned off) { return *(const bf16x8*)(sm + off); }
DI bf16x4 lds_tr(const char* sm, unsigned off) {
  return __builtin_amdgcn_ds_read_tr16_b64_v4bf16((LDS_AS bf16x4*)(LDS_AS char*)(sm + off));
}
DI bf16x8 cat8(bf16x4 lo, bf16x4 hi) { return __builtin_shufflevector(lo, hi, 0, 1, 2, 3, 4, 5, 6, 7); }
DI bf16x8 pack_lo(const f32x16& v) { return __builtin_convertvector(__builtin_shufflevector(v, v, 0, 1, 2, 3, 4, 5, 6, 7), bf16x8); }
DI bf16x8 pack_hi(const f32x16& v) { return __builtin_convertvector(__builtin_shufflevector(v, v, 8, 9, 10, 11, 12, 13, 14, 15), bf16x8); }
DI float bf_lo(unsigned u) { return __uint_as_float(u << 16); }
DI float bf_hi(unsigned u) { return __uint_as_float(u & 0xffff0000u); }
DI int vslot(int d, int h) {
  int s = (d < 4) ? d : (d >= 12) ? d - 8 : (d < 11) ? d + 4 : -1;
  return (s >= 0) ? (16 * h + s) : (32 + h);
}

DI LDS_AS unsigned* lds_word(char* sm, int off) { return (LDS_AS unsigned*)(LDS_AS char*)(sm + off); }
DI void flag_add(char* sm, int off, int lane) {
  if (lane == 0) __hip_atomic_fetch_add(lds_word(sm, off), 1u, __ATOMIC_RELAXED, __HIP_MEMORY_SCOPE_WORKGROUP);
  asm volatile("" ::: "memory");
}
DI unsigned flag_peek(char* sm, int off) { return __hip_atomic_load(lds_word(sm, off), __ATOMIC_RELAXED, __HIP_MEMORY_SCOPE_WORKGROUP); }
template <int SLEEP = 1>
DI void flag_wait(char* sm, int off, unsigned target) {
  while ((unsigned)__builtin_amdgcn_readfirstlane(flag_peek(sm, off)) < target) __builtin_amdgcn_s_sleep(SLEEP);
  asm volatile("" ::: "memory");
}

constexpr int K1_SLOT = 28672, K1_WOFF = 16384, K1_NSLOT = 5;
constexpr int K1_STATS = K1_SLOT * K1_NSLOT;
constexpr int K1_WPART = K1_STATS + 1024;
constexpr int K1_FLG = K1_WPART + 768;
constexpr int K1_SIDE = K1_FLG + 256;
constexpr int K1_LDS = K1_SIDE + 16384;
#define K1_READY(s) (K1_FLG + 4 * (s))
#define K1_DONE(s) (K1_FLG + 32 + 4 * (s))
#define K1_FIN (K1_FLG + 44)

template <int NV>
DI void k1_gemm_wave(char* sm, int w, int lane, int rg, int cg, char* ws) {
  const int h = lane >> 5, r31 = lane & 31;
  unsigned aoff[4];
  {
    const int row = 32 * w + r31;
#pragma unroll
    for (int ks = 0; ks < 4; ++ks) aoff[ks] = row * 128 + (((2 * ks + h) ^ ((row >> 1) & 7)) << 4);
  }
  const unsigned boff = K1_WOFF + (8 * h + ((lane & 15) >> 2)) * 192 + (16 * ((lane >> 4) & 1) + 4 * (lane & 3)) * 2;
  f32x16 acc[3] = {};
  bf16x8 fa[4], fb[4][3];
#pragma unroll
  for (int i = 2; i < 4; ++i) {
    fa[i] = bf16x8{};
#pragma unroll
    for (int j = 0; j < 3; ++j) fb[i][j] = bf16x8{};
  }
#define K1_MMA(ps)                                                        \
  _Pragma("unroll") for (int j = 0; j < 3; ++j) {                         \
    if (j >= 3 - NV) acc[j] = MFMA(fa[ps], fb[ps][j], acc[j]);   \
    else acc[j] = MFMA(fb[ps][j], fa[ps], acc[j]);               \
  }
  int slot = 0;
  unsigned rdy = 0;
#pragma nounroll
  for (int s = 0; s < 8; ++s) {
    if ((unsigned)__builtin_amdgcn_readfirstlane(rdy) < 4u) flag_wait(sm, K1_READY(s), 4);
    asm volatile("" ::: "memory");
    const char* sl = sm + slot;
#pragma unroll
    for (int ks = 0; ks < 4; ++ks) {
      if (ks == 2) rdy = flag_peek(sm, K1_READY(s + 1));
      fa[ks] = lds_b128(sl, aoff[ks]);
#pragma unroll
      for (int j = 0; j < 3; ++j)
        fb[ks][j] = cat8(lds_tr(sl, boff + ks * 16 * 192 + 64 * j), lds_tr(sl, boff + (ks * 16 + 4) * 192 + 64 * j));
      __builtin_amdgcn_sched_barrier(0);
      K1_MMA((ks + 2) & 3);
      __builtin_amdgcn_sched_barrier(0);
      if (ks == 1 && s >= 1 && s <= 3) flag_add(sm, K1_DONE(s - 1), lane);
    }
    slot += K1_SLOT;
    if (slot == K1_NSLOT * K1_SLOT) slot = 0;
  }
  K1_MMA(2);
  K1_MMA(3);
  asm volatile("" : "+v"(acc[0]), "+v"(acc[1]), "+v"(acc[2]));
  flag_wait(sm, K1_FIN, 4);
  const int rt = rg * 4 + w;
  const __amdgpu_buffer_rsrc_t rs = ws_rsrc(ws);
  const f32x2* stats = (const f32x2*)(sm + K1_STATS) + 32 * w;
  const float* wsum = (const float*)(sm + K1_WPART);
#pragma unroll
  for (int j = 0; j < 3; ++j) {
    const int ct = cg * 3 + j, role = ct >> 4, hp = (ct & 15) * 2;
    f32x16 v;
    if (j >= 3 - NV) {
      const float wsn = wsum[32 * j + r31] + wsum[96 + 32 * j + r31];
#pragma unroll
      for (int g = 0; g < 4; ++g) {
        f32x4 s0 = *(const f32x4*)(stats + 8 * g + 4 * h), s1 = *(const f32x4*)(stats + 8 * g + 4 * h + 2);
        v[4 * g + 0] = acc[j][4 * g + 0] * s0[0] + s0[1] * wsn;
        v[4 * g + 1] = acc[j][4 * g + 1] * s0[2] + s0[3] * wsn;
        v[4 * g + 2] = acc[j][4 * g + 2] * s1[0] + s1[1] * wsn;
        v[4 * g + 3] = acc[j][4 * g + 3] * s1[2] + s1[3] * wsn;
      }
      const unsigned base = (unsigned)WS_KV + (hp + (r31 >> 4)) * KVHEAD + rt * KVTILE + 1024;
      const int sl = vslot(r31 & 15, h);
      st16(rs, base + sl * 16, pack_lo(v));
      st16(rs, base + VBLK + sl * 16, pack_hi(v));
      if ((lane & 15) == 0)
        st16(rs, base + h * VBLK + 15 * 16, u32x4{0x3f803f80u, 0x3f803f80u, 0x3f803f80u, 0x3f803f80u});
    } else {
      f32x2 st = stats[r31];
      if (role == 0) { st[0] *= QSCALE; st[1] *= QSCALE; }
#pragma unroll
      for (int g = 0; g < 4; ++g) {
        f32x4 wsv = *(const f32x4*)(wsum + 32 * j + 8 * g + 4 * h) + *(const f32x4*)(wsum + 96 + 32 * j + 8 * g + 4 * h);
#pragma unroll
        for (int e = 0; e < 4; ++e) v[4 * g + e] = acc[j][4 * g + e] * st[0] + st[1] * wsv[e];
      }
      if (role == 0) {
        const unsigned base = (unsigned)WS_QF + (hp * 64 + rt) * 1024 + lane * 16;
        st16(rs, base, pack_lo(v));
        st16(rs, base + 64 * 1024, pack_hi(v));
      } else {
        const unsigned base = (unsigned)WS_KV + hp * KVHEAD + rt * KVTILE + lane * 16;
        st16(rs, base, pack_lo(v));
        st16(rs, base + KVHEAD, pack_hi(v));
      }
    }
  }
}


#define MFMA16(a, b, c) __builtin_amdgcn_mfma_f32_16x16x32_bf16((a), (b), (c), 0, 0, 0)
DI void k1_stats_wave(char* sm, const char* __restrict__ xg, int hw, int lane, int rg, int cg, char* ws) {
  const int r15 = lane & 15, kq = lane >> 4;
  unsigned aoff[4];
#pragma unroll
  for (int i = 0; i < 4; ++i) {
    const int row = 32 * hw + 16 * (i >> 1) + r15, c = 4 * (i & 1) + kq;
    aoff[i] = row * 128 + ((c ^ ((row >> 1) & 7)) << 4);
  }
  const unsigned boff = K1_WOFF + (32 * (hw & 1) + 8 * kq + (r15 >> 2)) * 192 + (48 * (hw >> 1) + 4 * (lane & 3)) * 2;
  const bf16x8 ones = __builtin_bit_cast(bf16x8, u32x4{0x3f803f80u, 0x3f803f80u, 0x3f803f80u, 0x3f803f80u});
  f32x4 gram[2] = {}, rsum[2] = {}, wacc[3] = {};
  bf16x8 xa[2][4] = {}, wb[2][3] = {};
  int slot = 0;
  u32x4 xr2[2];
#pragma unroll
  for (int i = 0; i < 2; ++i)
    xr2[i] = *(const u32x4*)(xg + (size_t)(rg * 128 + cg * 8 + (lane & 7)) * 1024 + ((lane >> 3) + 8 * (2 * hw + i)) * 16);
  auto math = [&](const bf16x8 (&xp)[4], const bf16x8 (&wp)[3]) {
#pragma unroll
    for (int i = 0; i < 4; ++i) {
      gram[i >> 1] = MFMA16(xp[i], xp[i], gram[i >> 1]);
      rsum[i >> 1] = MFMA16(xp[i], ones, rsum[i >> 1]);
    }
#pragma unroll
    for (int j = 0; j < 3; ++j) wacc[j] = MFMA16(ones, wp[j], wacc[j]);
  };
#pragma nounroll
  for (int s2 = 0; s2 < 4; ++s2) {
#pragma unroll
    for (int par = 0; par < 2; ++par) {
      flag_wait(sm, K1_READY(2 * s2 + par), 4);
      const char* sl = sm + slot;
#pragma unroll
      for (int i = 0; i < 4; ++i) xa[par][i] = lds_b128(sl, aoff[i]);
#pragma unroll
      for (int j = 0; j < 3; ++j) wb[par][j] = cat8(lds_tr(sl, boff + 32 * j), lds_tr(sl, boff + 4 * 192 + 32 * j));
      __builtin_amdgcn_sched_barrier(0);
      math(xa[par ^ 1], wb[par ^ 1]);
      __builtin_amdgcn_sched_barrier(0);
      WAIT_LGKM0();
      if (2 * s2 + par <= 2) flag_add(sm, K1_DONE(2 * s2 + par), lane);
      slot += K1_SLOT;
      if (slot == K1_NSLOT * K1_SLOT) slot = 0;
    }
  }
  math(xa[1], wb[1]);
  if (lane < 16) {
#pragma unroll
    for (int j = 0; j < 3; ++j) *(float*)(sm + K1_WPART + ((hw & 1) * 96 + 48 * (hw >> 1) + 16 * j + lane) * 4) = wacc[j][0];
  }
  if ((r15 >> 2) == kq) {
#pragma unroll
    for (int g2 = 0; g2 < 2; ++g2) {
      const int e = r15 & 3;
      const float sq = e == 0 ? gram[g2][0] : e == 1 ? gram[g2][1] : e == 2 ? gram[g2][2] : gram[g2][3];
      const float sum = e == 0 ? rsum[g2][0] : e == 1 ? rsum[g2][1] : e == 2 ? rsum[g2][2] : rsum[g2][3];
      const int row = 32 * hw + 16 * g2 + r15;
      const float mean = sum * (1.f / 512.f);
      float var = sq * (1.f / 512.f) - mean * mean;
      if (__builtin_expect(!(var > 1e-3f * mean * mean), 0)) {
        const unsigned* xr = (const unsigned*)(xg + (size_t)(rg * 128 + row) * 1024);
        float acc = 0.f;
        for (int i = 0; i < 256; ++i) {
          const unsigned u = xr[i];
          const float a = bf_lo(u) - mean, b = bf_hi(u) - mean;
          acc = fmaf(a, a, fmaf(b, b, acc));
        }
        var = acc * (1.f / 512.f);
      }
      const float rstd = __builtin_amdgcn_rsqf(var + 1e-5f);
      *(f32x2*)(sm + K1_STATS + row * 8) = f32x2{rstd, -mean * rstd};
    }
  }
  WAIT_LGKM0();
  flag_add(sm, K1_FIN, lane);
  flag_wait(sm, K1_FIN, 4);
  {
    const int p = lane & 7, lrow = cg * 8 + p;
    const f32x2 st = *(const f32x2*)(sm + K1_STATS + lrow * 8);
    const int grow = rg * 128 + lrow;
#pragma unroll
    for (int i = 0; i < 2; ++i) {
      const int c = (lane >> 3) + 8 * (2 * hw + i);
      const u32x4 u = xr2[i];
      f32x8 v;
#pragma unroll
      for (int e = 0; e < 4; ++e) { v[2 * e] = bf_lo(u[e]) * st[0] + st[1]; v[2 * e + 1] = bf_hi(u[e]) * st[0] + st[1]; }
      st16(ws_rsrc(ws), (unsigned)WS_XN + (((grow >> 5) * 32 + (c >> 1)) * 64 + (c & 1) * 32 + (grow & 31)) * 16,
           __builtin_convertvector(v, bf16x8));
    }
  }
}

DI void k1_loader_wave(char* sm, unsigned sbase, const char* xg, const char* wqkv, const char* w1, const char* w2,
                       const char* wo, int lw, int lane, int bid, int rg, int cg, int rot, char* ws) {
  unsigned xoff[4], woff[3];
#pragma unroll
  for (int p = 0; p < 4; ++p) {
    int c = (lane & 7) ^ ((4 * p + (lane >> 4)) & 7);
    xoff[p] = (unsigned)((rg * 128 + 32 * lw + 8 * p + (lane >> 3)) * 1024 + c * 16);
  }
#pragma unroll
  for (int i = 0; i < 3; ++i) {
    int idx = 64 * (3 * lw + i) + lane;
    int row = idx / 12, ch = idx - row * 12;
    woff[i] = (unsigned)(row * 3072 + cg * 192 + ch * 16);
  }
  auto issue = [&](int s, int slot) {
    const int ks = (s + rot) & 7;
    const unsigned dst = sbase + slot;
#pragma unroll
    for (int i = 0; i < 4; ++i) glds16(xg + ks * 128, xoff[i], dst + lw * 4096 + i * 1024);
#pragma unroll
    for (int i = 0; i < 3; ++i) glds16(wqkv + (size_t)ks * 64 * 3072, woff[i], dst + K1_WOFF + lw * 3072 + i * 1024);
  };
  issue(0, 0);
  issue(1, K1_SLOT);
  const char* swsrc; int sld, kb, nb, sks; size_t sdst; bool perm;
  if (bid < 112) { swsrc = w1; sld = FF_ * 2; kb = bid / 14; nb = bid - kb * 14; sks = 32; sdst = WS_W1; perm = false; }
  else if (bid < 224) { int v = bid - 112; swsrc = w2; sld = D_ * 2; kb = v >> 2; nb = v & 3; sks = 112; sdst = WS_W2; perm = true; }
  else { int v = bid - 224; swsrc = wo; sld = D_ * 2; kb = v >> 2; nb = v & 3; sks = 32; sdst = WS_WO; perm = true; }
  const int h = lane >> 5, q = (lane & 15) >> 2;
  const unsigned cb = (16 * ((lane >> 4) & 1) + 4 * (lane & 3)) * 2;
  {
    int islot = 2 * K1_SLOT;
#pragma nounroll
    for (int s = 2; s < 8; ++s) {
      wait_vm<7>(); flag_add(sm, K1_READY(s - 2), lane);
      if (s >= 5) flag_wait(sm, K1_DONE(s - 5), 8);
      issue(s, islot);
      islot += K1_SLOT;
      if (islot == K1_NSLOT * K1_SLOT) islot = 0;
    }
  }
  wait_vm<7>(); flag_add(sm, K1_READY(6), lane);
#pragma unroll
  for (int i = 0; i < 4; ++i) {
    const int p = 4 * lw + i;
    glds16(swsrc + (size_t)(kb * 64 + 4 * p) * sld + nb * 256, (unsigned)((lane >> 4) * sld + (lane & 15) * 16),
           sbase + K1_SIDE + p * 1024);
  }
  wait_vm<4>(); flag_add(sm, K1_READY(7), lane);
  WAIT_VM(0);
  {
    const int r0 = perm ? 4 * h : 8 * h, r1 = perm ? 8 + 4 * h : 8 * h + 4;
    const unsigned o0 = K1_SIDE + (16 * lw + r0 + q) * 256 + cb, o1 = K1_SIDE + (16 * lw + r1 + q) * 256 + cb;
#pragma unroll
    for (int nt = 0; nt < 4; ++nt) {
      bf16x8 f = cat8(lds_tr(sm, o0 + nt * 64), lds_tr(sm, o1 + nt * 64));
      st16(ws_rsrc(ws), (unsigned)sdst + ((nb * 4 + nt) * sks + kb * 4 + lw) * 1024 + lane * 16, f);
    }
  }
}

extern "C" __global__ __launch_bounds__(768) void k1_qkv(const char* __restrict__ x, const char* __restrict__ wqkv,
                                              const char* __restrict__ w1, const char* __restrict__ w2,
                                              const char* __restrict__ wo, char* __restrict__ ws) {
  extern __shared__ __attribute__((aligned(16))) char sm[];
  const int lane = threadIdx.x & 63, wave = __builtin_amdgcn_readfirstlane(threadIdx.x >> 6);
  const int bid = blockIdx.x, xcd = bid & 7, idx = bid >> 3;
  const int rg = 4 * (xcd >> 1) + (idx & 3), cg = 8 * (xcd & 1) + (idx >> 2);
  if (threadIdx.x >= 256 && threadIdx.x < 272) *(unsigned*)(sm + K1_FLG + (threadIdx.x - 256) * 4) = 0u;
  WAIT_LGKM0();
  BARRIER();
  if (wave < 4) {
    const int rot = ((idx >> 2) + 2 * (idx & 3)) & 7;
    k1_loader_wave(sm, lds_addr(sm), x, wqkv, w1, w2, wo, wave, lane, bid, rg, cg, rot, ws);
  } else if (wave < 8) {
    if (cg < 10) k1_gemm_wave<0>(sm, wave - 4, lane, rg, cg, ws);
    else if (cg == 10) k1_gemm_wave<1>(sm, wave - 4, lane, rg, cg, ws);
    else k1_gemm_wave<3>(sm, wave - 4, lane, rg, cg, ws);
  } else {
    if (bid == 0) st16(ws_rsrc(ws), (unsigned)WS_FLAG + (wave - 8) * 1024 + lane * 16, u32x4{0u, 0u, 0u, 0u});
#pragma unroll
    for (int i = 0; i < 2; ++i)
      st16(ws_rsrc(ws), (unsigned)WS_OF + bid * 8192 + (wave - 8) * 2048 + i * 1024 + lane * 16, u32x4{~0u, ~0u, ~0u, ~0u});
    k1_stats_wave(sm, x, wave - 8, lane, rg, cg, ws);
  }
}

constexpr int K2_FLG = KVHEAD;
constexpr int K2_LDS = K2_FLG + 64;
constexpr float SM_DELTA = 80.f;
typedef __attribute__((address_space(1))) unsigned gu32;
#define SB() __builtin_amdgcn_sched_barrier(0)

DI float xhalf_max(float v) {
  auto r = __builtin_amdgcn_permlane32_swap(__float_as_uint(v), __float_as_uint(v), false, false);
  return fmaxf(__uint_as_float(r[0]), __uint_as_float(r[1]));
}
DI float max16(const f32x16& s) {
  float m01 = fmaxf(fmaxf(s[0], s[1]), s[2]), m23 = fmaxf(fmaxf(s[3], s[4]), s[5]);
  float m45 = fmaxf(fmaxf(s[6], s[7]), s[8]), m67 = fmaxf(fmaxf(s[9], s[10]), s[11]);
  float m89 = fmaxf(fmaxf(s[12], s[13]), s[14]);
  return fmaxf(fmaxf(fmaxf(m01, m23), m45), fmaxf(fmaxf(m67, m89), s[15]));
}
DI void lds_signal(char* sm, int word, int lane) { flag_add(sm, K2_FLG + 4 * word, lane); }
DI void lds_wait(char* sm, int word, unsigned target) { flag_wait(sm, K2_FLG + 4 * word, target); }

template <int NKS, int PF, int AUXB>
DI void dgemm64_ring(__amdgpu_buffer_rsrc_t rs, unsigned a0, unsigned a1, unsigned b0, unsigned b1, int rot, f32x16 (&acc)[4]) {
  u32x4 A0[PF], A1[PF], B0[PF], B1[PF];
  int ko = rot * 1024;
#pragma unroll
  for (int kk = 0; kk < NKS + PF - 1; ++kk) {
    if (kk < NKS) {
      const int sl = kk % PF;
      A0[sl] = __builtin_amdgcn_raw_buffer_load_b128(rs, a0, ko, 0);
      B0[sl] = __builtin_amdgcn_raw_buffer_load_b128(rs, b0, ko, AUXB);
      A1[sl] = __builtin_amdgcn_raw_buffer_load_b128(rs, a1, ko, 0);
      B1[sl] = __builtin_amdgcn_raw_buffer_load_b128(rs, b1, ko, AUXB);
      ko += 1024;
      if (ko == NKS * 1024) ko = 0;
    }
    SB();
    if (kk >= PF - 1) {
      const int sl = (kk - (PF - 1)) % PF;
      const bf16x8 fa0 = __builtin_bit_cast(bf16x8, A0[sl]), fa1 = __builtin_bit_cast(bf16x8, A1[sl]);
      const bf16x8 fb0 = __builtin_bit_cast(bf16x8, B0[sl]), fb1 = __builtin_bit_cast(bf16x8, B1[sl]);
      acc[0] = MFMA(fa0, fb0, acc[0]);
      SB(); __builtin_amdgcn_s_sleep(1); SB();
      acc[2] = MFMA(fa1, fb0, acc[2]);
      SB(); __builtin_amdgcn_s_sleep(1); SB();
      acc[1] = MFMA(fa0, fb1, acc[1]);
      SB(); __builtin_amdgcn_s_sleep(1); SB();
      acc[3] = MFMA(fa1, fb1, acc[3]);
    }
    SB();
  }
  asm volatile("" : "+v"(acc[0]), "+v"(acc[1]), "+v"(acc[2]), "+v"(acc[3]));
}

DI void k2_ffn_wave(char* sm, const char* __restrict__ x, char* __restrict__ ws, char* __restrict__ out, int w, int lane, int panel, int ci, int head) {
  {
    const unsigned sb = lds_addr(sm) + w * 1024, dl = lane * 16;
    const char* kvg = ws + WS_KV + (size_t)head * KVHEAD + w * 1024;
#pragma unroll
    for (int i = 0; i < 8; ++i) glds16(kvg + i * 4096, dl, sb + i * 4096);
    WAIT_LGKM0();
    BARRIER();
    WAIT_VM(4);
    lds_signal(sm, 0, lane);
#pragma unroll
    for (int i = 8; i < 16; ++i) glds16(kvg + i * 4096, dl, sb + i * 4096);
    WAIT_VM(4);
    lds_signal(sm, 1, lane);
#pragma unroll
    for (int i = 16; i < 34; ++i) glds16(kvg + i * 4096, dl, sb + i * 4096);
    WAIT_VM(0);
    lds_signal(sm, 2, lane);
  }
  __builtin_amdgcn_s_setprio(2);
  const __amdgpu_buffer_rsrc_t rs = __builtin_amdgcn_make_buffer_rsrc((void*)ws, 0, (int)WS_END, 0x00020000);
  gu32* flags = (gu32*)(ws + WS_FLAG) + panel * 128;
  if (ci < 28) {
    const int rt0 = panel * 8 + 2 * w, nt0 = 2 * ci;
    const unsigned a0 = (unsigned)WS_W1 + nt0 * 32768 + lane * 16, b0 = (unsigned)WS_XN + rt0 * 32768 + lane * 16;
    f32x16 acc[4] = {};
    dgemm64_ring<32, 5, 0>(rs, a0, a0 + 32768, b0, b0 + 32768, ci, acc);
#pragma unroll
    for (int i = 0; i < 2; ++i)
#pragma unroll
      for (int j = 0; j < 2; ++j) {
        f32x16 v = acc[2 * i + j];
#pragma unroll
        for (int e = 0; e < 16; ++e) v[e] = fmaxf(v[e], 0.f);
        const unsigned dst = (unsigned)WS_HF + ((rt0 + j) * 112 + (nt0 + i) * 2) * 1024 + lane * 16;
        __builtin_amdgcn_raw_buffer_store_b128(__builtin_bit_cast(u32x4, pack_lo(v)), rs, dst, 0, 16);
        __builtin_amdgcn_raw_buffer_store_b128(__builtin_bit_cast(u32x4, pack_hi(v)), rs, dst + 1024, 0, 16);
      }
    WAIT_VM(0);
    if (lane == 0) __hip_atomic_store(flags + w * 32 + ci, 1u, __ATOMIC_RELAXED, __HIP_MEMORY_SCOPE_AGENT);
  }
  const int rp2 = ci & 3, np2 = ci >> 2;
  {
    __builtin_amdgcn_s_setprio(0);
    gu32* f = flags + rp2 * 32 + 7 * w + min(lane, 6);
    for (unsigned spins = 0; spins < (1u << 20); ++spins) {
      unsigned v = __hip_atomic_load(f, __ATOMIC_RELAXED, __HIP_MEMORY_SCOPE_AGENT);
      if (__all(v == 1u)) break;
      __builtin_amdgcn_s_sleep(8);
    }
    __builtin_amdgcn_fence(__ATOMIC_ACQUIRE, "wavefront");
    asm volatile("" ::: "memory");
  }
  __builtin_amdgcn_s_setprio(2);
  const int rt0 = panel * 8 + 2 * rp2, nt0 = 2 * np2;
  f32x16 acc[4] = {};
  {
    const unsigned a0 = (unsigned)WS_W2 + (nt0 * 112 + 28 * w) * 1024 + lane * 16;
    const unsigned b0 = (unsigned)WS_HF + (rt0 * 112 + 28 * w) * 1024 + lane * 16;
    dgemm64_ring<28, 5, 16>(rs, a0, a0 + 112 * 1024, b0, b0 + 112 * 1024, (7 * rp2 + 3 * np2) % 28, acc);
  }
  const int h = lane >> 5, r31 = lane & 31;
  const unsigned eoff = (unsigned)(((rt0 + (w & 1)) * 32 + r31) * D_ + (nt0 + (w >> 1)) * 32 + 16 * h) * 2;
  u32x4 xv[2];
#pragma unroll
  for (int c = 0; c < 2; ++c) xv[c] = *(const u32x4*)(x + eoff + 16 * c);
  __builtin_amdgcn_s_setprio(0);
  flag_wait<6>(sm, K2_FLG + 12, 8);
  {
    const int wl = 65536 + w * 16384;
    const char* wog = ws + WS_WO + (size_t)(nt0 * 32 + 8 * w) * 1024;
#pragma unroll
    for (int i = 0; i < 16; ++i) glds16(wog + (i >> 3) * 32768 + (i & 7) * 1024, lane * 16, lds_addr(sm) + wl + i * 1024);
    gu32* f = (gu32*)(ws + WS_OF + ((rt0 + ((lane >> 3) & 1)) * 32 + 8 * w + (lane & 7)) * 1024 + 1020);
    for (unsigned spins = 0; spins < (1u << 20); ++spins) {
      unsigned v = __hip_atomic_load(f, __ATOMIC_RELAXED, __HIP_MEMORY_SCOPE_AGENT);
      if (__all(v != ~0u)) break;
      __builtin_amdgcn_s_sleep(2);
    }
    asm volatile("" ::: "memory");
    const unsigned b0 = (unsigned)WS_OF + (rt0 * 32 + 8 * w) * 1024 + lane * 16;
    u32x4 B[16];
    for (unsigned spins = 0; spins < (1u << 16); ++spins) {
      asm volatile("" ::: "memory");
#pragma unroll
      for (int i = 0; i < 16; ++i) B[i] = __builtin_amdgcn_raw_buffer_load_b128(rs, b0, (i & 1) * 32768 + (i >> 1) * 1024, 16);
      unsigned m = 0;
#pragma unroll
      for (int i = 0; i < 16; ++i) m = max(max(m, B[i][0]), max(B[i][1], max(B[i][2], B[i][3])));
      if (__builtin_expect(!__any(m == ~0u), 1)) break;
    }
    WAIT_VM(0);
#pragma unroll
    for (int ks = 0; ks < 8; ++ks) {
      const bf16x8 fa0 = lds_b128(sm, wl + ks * 1024 + lane * 16), fa1 = lds_b128(sm, wl + 8192 + ks * 1024 + lane * 16);
      const bf16x8 fb0 = __builtin_bit_cast(bf16x8, B[2 * ks]), fb1 = __builtin_bit_cast(bf16x8, B[2 * ks + 1]);
      acc[0] = MFMA(fa0, fb0, acc[0]);
      acc[2] = MFMA(fa1, fb0, acc[2]);
      acc[1] = MFMA(fa0, fb1, acc[1]);
      acc[3] = MFMA(fa1, fb1, acc[3]);
    }
  }
  {
    f32x4* red = (f32x4*)sm + lane;
#pragma unroll
    for (int t = 0; t < 4; ++t)
#pragma unroll
      for (int g = 0; g < 4; ++g)
        red[((t * 4 + w) * 4 + g) * 64] = f32x4{acc[t][4 * g], acc[t][4 * g + 1], acc[t][4 * g + 2], acc[t][4 * g + 3]};
    WAIT_LGKM0();
    lds_signal(sm, 4, lane);
    lds_wait(sm, 4, 4);
    const f32x4* rd = (const f32x4*)sm + r31;
    const __amdgpu_buffer_rsrc_t ro = __builtin_amdgcn_make_buffer_rsrc((void*)out, 0, S_ * D_ * 2, 0x00020000);
#pragma unroll
    for (int c = 0; c < 2; ++c) {
      const int g = 2 * h + c;
      f32x4 lo = rd[((w * 4 + 0) * 4 + g) * 64], hi = rd[((w * 4 + 0) * 4 + g) * 64 + 32];
#pragma unroll
      for (int src = 1; src < 4; ++src) {
        lo += rd[((w * 4 + src) * 4 + g) * 64];
        hi += rd[((w * 4 + src) * 4 + g) * 64 + 32];
      }
      f32x8 v;
      v[0] = lo[0] + bf_lo(xv[c][0]); v[1] = lo[1] + bf_hi(xv[c][0]); v[2] = lo[2] + bf_lo(xv[c][1]); v[3] = lo[3] + bf_hi(xv[c][1]);
      v[4] = hi[0] + bf_lo(xv[c][2]); v[5] = hi[1] + bf_hi(xv[c][2]); v[6] = hi[2] + bf_lo(xv[c][3]); v[7] = hi[3] + bf_hi(xv[c][3]);
      __builtin_amdgcn_raw_buffer_store_b128(__builtin_bit_cast(u32x4, __builtin_convertvector(v, bf16x8)), ro, eoff + 16 * c, 0, 0);
    }
  }
}

extern "C" __global__ __launch_bounds__(768) void k2_attn(const char* __restrict__ x, char* __restrict__ ws, char* __restrict__ out) {
  extern __shared__ __attribute__((aligned(16))) char sm[];
  const int lane = threadIdx.x & 63, wave = __builtin_amdgcn_readfirstlane(threadIdx.x >> 6);
  const int bid = blockIdx.x, xcd = bid & 7, idx = bid >> 3;
  if (threadIdx.x < 16) *(unsigned*)(sm + K2_FLG + threadIdx.x * 4) = 0u;
  const int head = xcd * 4 + (idx >> 3), qb = idx & 7, rt = qb * 8 + wave;
  if (wave >= 8) {
    k2_ffn_wave(sm, x, ws, out, wave - 8, lane, xcd, idx, head);
    return;
  }
  const bf16x8 qf = *((const bf16x8*)(ws + WS_QF) + ((size_t)(head * 64 + rt) * 64 + lane));
  const int h = lane >> 5, r31 = lane & 31;
  const unsigned koff = lane * 16;
  const unsigned voff = 1024 + ((r31 < 16) ? vslot(r31, h) : 15) * 16;
  WAIT_LGKM0();
  BARRIER();
  f32x16 negm = {}, O = {};
#define EXP(v, i) v[i] = __builtin_amdgcn_exp2f(v[i])
#define TILE_STEP(SC, SN, OFFK2, OFFV0N, OFFV1)                                   \
  {                                                                               \
    SN = MFMA(kn, qf, negm);                                                      \
    SB();                                                                         \
    kn = lds_b128(kp, OFFK2);                                                     \
    EXP(SC, 0); EXP(SC, 1); EXP(SC, 2); EXP(SC, 3); EXP(SC, 4); EXP(SC, 5);       \
    SB();                                                                         \
    O = MFMA(v1p, p1p, O);                                                        \
    SB();                                                                         \
    v1p = lds_b128(vp, OFFV1);                                                    \
    EXP(SC, 6); EXP(SC, 7);                                                       \
    bf16x8 p0 = pack_lo(SC);                                                      \
    SB();                                                                         \
    EXP(SC, 8); EXP(SC, 9);                                                       \
    SB();                                                                         \
    O = MFMA(v0, p0, O);                                                          \
    SB();                                                                         \
    v0 = lds_b128(vp, OFFV0N);                                                    \
    EXP(SC, 10); EXP(SC, 11); EXP(SC, 12); EXP(SC, 13); EXP(SC, 14); EXP(SC, 15); \
    p1p = pack_hi(SC);                                                            \
    SB();                                                                         \
  }
#define TILE4()                                                        \
  TILE_STEP(sA, sB, 2 * KVTILE, 1 * KVTILE, 0 * KVTILE + VBLK);        \
  TILE_STEP(sB, sA, 3 * KVTILE, 2 * KVTILE, 1 * KVTILE + VBLK);        \
  TILE_STEP(sA, sB, 4 * KVTILE, 3 * KVTILE, 2 * KVTILE + VBLK);        \
  TILE_STEP(sB, sA, 5 * KVTILE, 4 * KVTILE, 3 * KVTILE + VBLK);        \
  kp += 4 * KVTILE; vp += 4 * KVTILE;
  {
    const char* kp = sm + koff;
    const char* vp = sm + voff;
    lds_wait(sm, 0, 4);
    const bf16x8 k0 = lds_b128(kp, 0);
    bf16x8 kn = lds_b128(kp, KVTILE), v0 = lds_b128(vp, 0), v1p = lds_b128(vp, VBLK), p1p = {};
    {
      const f32x16 z = {};
      f32x16 s0 = MFMA(k0, qf, z), s1 = MFMA(kn, qf, z);
      const float nm = -(xhalf_max(fmaxf(max16(s0), max16(s1))) + SM_DELTA);
#pragma unroll
      for (int e = 0; e < 16; ++e) negm[e] = nm;
    }
    f32x16 sA = MFMA(k0, qf, negm), sB;
    int ev = 4;
    for (int t = 0; t < 64; t += 4) {
      if (__builtin_expect(t == ev, 0)) {
        lds_wait(sm, t == 4 ? 1 : 2, 4);
        if (t != 4 && wave >= 4) __builtin_amdgcn_s_setprio(1);
        ev = (t == 4) ? 16 : -100;
      }
      TILE4();
      asm volatile("" : "+v"(O), "+v"(sA), "+v"(p1p));
    }
    O = MFMA(v1p, p1p, O);
  }
  float l = O[8];
  bool ok = (l < 0x1p120f);
  float omax = 0.f;
#pragma unroll
  for (int e = 0; e < 8; ++e) { ok = ok && (fabsf(O[e]) < 0x1p126f); omax = fmaxf(omax, fabsf(O[e])); }
  ok = ok && (omax > 0x1p-100f);
  if (!__all(ok)) {
    float m = -INFINITY;
#pragma unroll
    for (int e = 0; e < 16; ++e) O[e] = 0.f;
    for (int t = 0; t < 64; ++t) {
      f32x16 z = {};
      f32x16 sc = MFMA(lds_b128(sm, koff + t * KVTILE), qf, z);
      bf16x8 v0 = lds_b128(sm, voff + t * KVTILE), v1 = lds_b128(sm, voff + t * KVTILE + VBLK);
      float mn = fmaxf(m, xhalf_max(max16(sc)));
      float alpha = __builtin_amdgcn_exp2f(m - mn);
      m = mn;
#pragma unroll
      for (int e = 0; e < 16; ++e) { O[e] *= alpha; sc[e] = __builtin_amdgcn_exp2f(sc[e] - mn); }
      O = MFMA(v0, pack_lo(sc), O);
      O = MFMA(v1, pack_hi(sc), O);
    }
    l = O[8];
  }
  const float rl = __builtin_amdgcn_rcpf(l);
#pragma unroll
  for (int e = 0; e < 8; ++e) O[e] *= rl;
  u32x4 po = __builtin_bit_cast(u32x4, pack_lo(O));
#pragma unroll
  for (int e = 0; e < 4; ++e) po[e] = min(po[e], 0xFFFFFFFEu);
  __builtin_amdgcn_raw_buffer_store_b128(po, ws_rsrc(ws), (unsigned)WS_OF + (rt * 32 + head) * 1024 + lane * 16, 0, 16);
  WAIT_LGKM0();
  lds_signal(sm, 3, lane);
}

extern "C" void kernel_launch(void* const* d_in, const int* in_sizes, int n_in, void* d_out, int out_size, void* d_ws,
                              size_t ws_size, hipStream_t stream) {
  const char* x = (const char*)d_in[0];
  const char* wqkv = (const char*)d_in[1];
  const char* wo = (const char*)d_in[2];
  const char* w1 = (const char*)d_in[4];
  const char* w2 = (const char*)d_in[5];
  char* ws = (char*)d_ws;
  (void)hipFuncSetAttribute((const void*)k1_qkv, hipFuncAttributeMaxDynamicSharedMemorySize, K1_LDS);
  (void)hipFuncSetAttribute((const void*)k2_attn, hipFuncAttributeMaxDynamicSharedMemorySize, K2_LDS);
  hipLaunchKernelGGL(k1_qkv, dim3(256), dim3(768), K1_LDS, stream, x, wqkv, w1, w2, wo, ws);
  hipLaunchKernelGGL(k2_attn, dim3(256), dim3(768), K2_LDS, stream, x, ws, (char*)d_out);
}
```
